```python
import torch

def _softmax(x, axis=-1):
    return torch.softmax(x, dim=axis)
def _gelu(x):
    return torch.nn.functional.gelu(x, approximate="tanh")
def _silu(x):
    return torch.nn.functional.silu(x)
def _sigmoid(x):
    return torch.sigmoid(x)
def _layernorm(x, axis=-1, eps=1e-5):
    m = x.mean(dim=axis, keepdim=True)
    v = x.var(dim=axis, keepdim=True, unbiased=False)
    return (x - m) / torch.sqrt(v + eps)
def _scatter_add(tgt, idx, src, axis=0):
    return tgt.clone().index_add_(axis, idx, src)
def _segment_sum(src, seg, n):
    out = torch.zeros((n,) + src.shape[1:], dtype=src.dtype)
    return out.index_add_(0, seg, src)
def _pad(x, pw):
    flat = tuple(v for pair in reversed(pw) for v in pair)
    return torch.nn.functional.pad(x, flat)

kernel_name = 'parallel_sum__spectral_fnet__dense_activation_squared_relu__prompt'

def setup_inputs():

    _w = (lambda a: (lambda t: t.to(torch.bfloat16) if t.is_floating_point() else t)(torch.as_tensor(a)))
    def _A_setup():
        B, S, D = 1, 2048, 512
        ns = torch.arange(S)[:, None] * torch.arange(S)[None, :]
        nd = torch.arange(D)[:, None] * torch.arange(D)[None, :]
        Ws_re = torch.cos(-2.0 * torch.pi * ns / S).to(float)
        Ws_im = torch.sin(-2.0 * torch.pi * ns / S).to(float)
        Wd_re = torch.cos(-2.0 * torch.pi * nd / D).to(float)
        Wd_im = torch.sin(-2.0 * torch.pi * nd / D).to(float)
        return {
            "x": _w((lambda *s: torch.randn(*s, dtype=torch.bfloat16) * 0.05)(B, S, D)),
            "dft_seq_re": _w(Ws_re),
            "dft_seq_im": _w(Ws_im),
            "dft_hid_re": _w(Wd_re),
            "dft_hid_im": _w(Wd_im),
        }
    _A_in = _A_setup()

    def _B_setup():
        B, S, D, F = 1, 2048, 512, 1792
        return {
            "x": _w((lambda *s: torch.randn(*s, dtype=torch.bfloat16) * 0.05)(B, S, D)),
            "w1": _w((lambda *s: torch.randn(*s, dtype=torch.bfloat16) * 0.2)(D, F)),
            "b1": _w((lambda *s: torch.randn(*s, dtype=torch.bfloat16) * 0.2)(F)),
            "w2": _w((lambda *s: torch.randn(*s, dtype=torch.bfloat16) * 0.2)(F, D)),
            "b2": _w((lambda *s: torch.randn(*s, dtype=torch.bfloat16) * 0.2)(D)),
        }
    _B_in = _B_setup()

    return {
        "A_x": _A_in["x"],
        "A_dft_seq_re": _A_in["dft_seq_re"],
        "A_dft_seq_im": _A_in["dft_seq_im"],
        "A_dft_hid_re": _A_in["dft_hid_re"],
        "A_dft_hid_im": _A_in["dft_hid_im"],
        "B_x": _B_in["x"],
        "B_w1": _B_in["w1"],
        "B_b1": _B_in["b1"],
        "B_w2": _B_in["w2"],
        "B_b2": _B_in["b2"],
    }

def reference(A_x, A_dft_seq_re, A_dft_seq_im, A_dft_hid_re, A_dft_hid_im, B_x, B_w1, B_b1, B_w2, B_b2):
    def _A_ref(x, dft_seq_re, dft_seq_im, dft_hid_re, dft_hid_im):
        a = torch.einsum("bsd,ed->bse", x, dft_hid_re)
        b = torch.einsum("bsd,ed->bse", x, dft_hid_im)
        y = torch.einsum("ts,bse->bte", dft_seq_re, a) - torch.einsum("ts,bse->bte", dft_seq_im, b)
        return y

    def _B_ref(x, w1, b1, w2, b2):
        h = torch.einsum("bsd,df->bsf", x, w1) + b1
        zero = torch.zeros_like(h)
        r = torch.where(h > zero, h, zero)
        h = r * r
        out = torch.einsum("bsf,fd->bsd", h, w2) + b2
        return out

    x = A_x
    xn = _layernorm(x, axis=-1)
    return x + _A_ref(xn, A_dft_seq_re, A_dft_seq_im, A_dft_hid_re, A_dft_hid_im) + _B_ref(xn, B_w1, B_b1, B_w2, B_b2)
```

```cpp
#include <hip/hip_runtime.h>
#include <hip/hip_bf16.h>
#include <stdint.h>

typedef unsigned short u16;
typedef __attribute__((ext_vector_type(8))) short bf16x8;
typedef __attribute__((ext_vector_type(4))) short bf16x4;
typedef __attribute__((ext_vector_type(2))) short bf16x2;
typedef __attribute__((ext_vector_type(4))) float f32x4;
typedef __attribute__((ext_vector_type(4))) unsigned int u32x4;
typedef __attribute__((ext_vector_type(2))) unsigned int u32x2;

#define SEQ 2048
#define DM 512
#define FF 1792
#define KTOT 5888

#define WAIT_V(n) asm volatile("s_waitcnt vmcnt(" #n ")" ::: "memory")
#define WAIT_L0() asm volatile("s_waitcnt lgkmcnt(0)" ::: "memory")
#define BAR() __builtin_amdgcn_s_barrier()

__device__ __forceinline__ float bf2f(u16 v) { return __uint_as_float(((unsigned)v) << 16); }
__device__ __forceinline__ float bf_lo(unsigned v) { return __uint_as_float(v << 16); }
__device__ __forceinline__ float bf_hi(unsigned v) { return __uint_as_float(v & 0xffff0000u); }
__device__ __forceinline__ u16 f2bf(float f) {
  __hip_bfloat16 h = __float2bfloat16(f);
  return __builtin_bit_cast(u16, h);
}
__device__ __forceinline__ float rbf(float f) { return bf2f(f2bf(f)); }
__device__ __forceinline__ unsigned pack2(float lo, float hi) {
  return (unsigned)f2bf(lo) | ((unsigned)f2bf(hi) << 16);
}

template <int N> __device__ __forceinline__ void wait_vm() {
  asm volatile("s_waitcnt vmcnt(%0)" ::"i"(N) : "memory");
}

__device__ __forceinline__ void glds16(const void* g, void* l) {
  __builtin_amdgcn_global_load_lds((const __attribute__((address_space(1))) unsigned*)g,
                                   (__attribute__((address_space(3))) unsigned*)l, 16, 0, 0);
}

#define K1_NBUF 6
#define K1_CHUNK 22528
#define K1_STATS (K1_NBUF * K1_CHUNK)
#define K1_LDS (K1_STATS + 1024)

template <int NAB, int WN>
__device__ __forceinline__ void k1_body(const u16* __restrict__ x, const u16* __restrict__ Wr,
                                        const u16* __restrict__ Wi, const u16* __restrict__ w1,
                                        const u16* __restrict__ b1, u16* __restrict__ BT,
                                        u16* __restrict__ H, char* smem, int mt, int nt) {
  constexpr int NH = 11 - NAB;
  constexpr int ROT = (NH == 11) ? 4 : 1;
  constexpr int NHD = (NH == 0) ? 1 : NH;
  const int tid = threadIdx.x;
  const int lane = tid & 63;
  const int w = __builtin_amdgcn_readfirstlane(tid >> 6);
  const int wm = w & 3;
  constexpr int wn = WN;
  const int li = lane & 15, g = lane >> 4;
  const int c0 = nt * 176;
  const int m0 = mt * 128 + wm * 32;

  bf16x8 xf[2][16];
#pragma unroll
  for (int mb = 0; mb < 2; ++mb) {
    const int row = m0 + 8 * (li >> 2) + 4 * mb + (li & 3);
    const u16* xp = x + row * DM + g * 8;
#pragma unroll
    for (int ks = 0; ks < 16; ++ks) xf[mb][ks] = *(const bf16x8*)(xp + ks * 32);
  }

  const u16* src[3];
  int inc[3];
#pragma unroll
  for (int jj = 0; jj < 3; ++jj) {
    int j = w + 8 * jj;
    if (j > 21) j = 21;
    if (j < 2 * NAB) {
      const int row = 8 * j + (lane >> 3);
      const int ch = (lane & 7) ^ ((row >> 1) & 7);
      const int n = c0 + row;
      const u16* base = (n < 512) ? (Wr + n * DM) : (Wi + (n - 512) * DM);
      src[jj] = base + ch * 8;
      inc[jj] = 64;
    } else {
      const int p16 = (j - 2 * NAB) * 64 + lane;
      const int r = p16 / (2 * NHD);
      const int rem = p16 - r * (2 * NHD);
      const int cp = rem >> 1, hf = rem & 1;
      int c = cp - ROT * ((r >> 3) & 1);
      if (c < 0) c += NH;
      src[jj] = w1 + r * FF + (c0 + 16 * NAB - 1024) + 16 * c + 8 * hf;
      inc[jj] = 64 * FF;
    }
  }
  const bool three = (w < 6);
  auto issue = [&](int kc) {
    char* dst = smem + (kc % K1_NBUF) * K1_CHUNK + w * 1024;
    glds16(src[0] + kc * inc[0], dst);
    glds16(src[1] + kc * inc[1], dst + 8192);
    if (three) glds16(src[2] + kc * inc[2], dst + 16384);
  };
  issue(0);
  issue(1);

  {
    float s = 0.f;
#pragma unroll
    for (int ks = 0; ks < 16; ++ks) {
      const u32x4 v = __builtin_bit_cast(u32x4, wn ? xf[1][ks] : xf[0][ks]);
#pragma unroll
      for (int q = 0; q < 4; ++q) s += bf_lo(v[q]) + bf_hi(v[q]);
    }
    issue(2);
    issue(3);
    issue(4);
    issue(5);
    s += __shfl_xor(s, 16);
    s += __shfl_xor(s, 32);
    const float mean = s * (1.0f / 512.0f);
    float vs = 0.f;
#pragma unroll
    for (int ks = 0; ks < 16; ++ks) {
      const u32x4 v = __builtin_bit_cast(u32x4, wn ? xf[1][ks] : xf[0][ks]);
#pragma unroll
      for (int q = 0; q < 4; ++q) {
        const float d0 = bf_lo(v[q]) - mean, d1 = bf_hi(v[q]) - mean;
        vs = fmaf(d0, d0, vs);
        vs = fmaf(d1, d1, vs);
      }
    }
    vs += __shfl_xor(vs, 16);
    vs += __shfl_xor(vs, 32);
    const float var = rbf(vs * (1.0f / 512.0f));
    const float sd = rbf(sqrtf(rbf(var + 1e-5f)));
    const float rinv = 1.0f / sd;
    const float mb16 = rbf(mean);
    if (g == 0) {
      float2* st = (float2*)(smem + K1_STATS);
      st[(wm * 2 + wn) * 16 + li] = make_float2(mb16, rinv);
    }
  }
  WAIT_L0();
  BAR();
#pragma unroll
  for (int mb = 0; mb < 2; ++mb) {
    const float2 st = ((const float2*)(smem + K1_STATS))[(wm * 2 + mb) * 16 + li];
#pragma unroll
    for (int ks = 0; ks < 16; ++ks) {
      u32x4 v = __builtin_bit_cast(u32x4, xf[mb][ks]);
#pragma unroll
      for (int q = 0; q < 4; ++q) {
        const float d0 = rbf(bf_lo(v[q]) - st.x), d1 = rbf(bf_hi(v[q]) - st.x);
        v[q] = pack2(d0 * st.y, d1 * st.y);
      }
      xf[mb][ks] = __builtin_bit_cast(bf16x8, v);
    }
  }

  constexpr int NB0 = 6;
  f32x4 acc[2][NB0];
#pragma unroll
  for (int mb = 0; mb < 2; ++mb)
#pragma unroll
    for (int b = 0; b < NB0; ++b) acc[mb][b] = (f32x4){0.f, 0.f, 0.f, 0.f};

  const int q4 = li >> 2, p4 = lane & 3;
#pragma unroll
  for (int kc = 0; kc < 8; ++kc) {
    if (kc >= 2) {
      if (three) {
        if (kc <= 4) wait_vm<9>(); else if (kc == 5) wait_vm<6>(); else if (kc == 6) wait_vm<3>(); else wait_vm<0>();
      } else {
        if (kc <= 4) wait_vm<6>(); else if (kc == 5) wait_vm<4>(); else if (kc == 6) wait_vm<2>(); else wait_vm<0>();
      }
      WAIT_L0();
      BAR();
      if (kc == 2) issue(6);
      if (kc == 3) issue(7);
    }
    const char* buf = smem + (kc % K1_NBUF) * K1_CHUNK;
#pragma unroll
    for (int ks = 0; ks < 2; ++ks) {
#pragma unroll
      for (int bb = 0; bb < NB0; ++bb) {
        if (wn == 1 && bb == 5) continue;
#pragma unroll
        for (int half = 0; half < 2; ++half) {
          if (half != wn) continue;
          const int b = half * NB0 + bb;
          if (b >= 11) continue;
          if (b < NAB) {
            const int row = 16 * b + li;
            const int ch = (4 * ks + g) ^ (li >> 1);
            const bf16x8 wf = *(const bf16x8*)(buf + row * 128 + ch * 16);
            acc[0][bb] = __builtin_amdgcn_mfma_f32_16x16x32_bf16(xf[0][kc * 2 + ks], wf, acc[0][bb], 0, 0, 0);
            acc[1][bb] = __builtin_amdgcn_mfma_f32_16x16x32_bf16(xf[1][kc * 2 + ks], wf, acc[1][bb], 0, 0, 0);
          } else {
            const int hb = b - NAB;
            int cp = hb + ROT * (g & 1);
            if (cp >= NH) cp -= NH;
            const char* hp = buf + NAB * 2048 + (32 * ks + 8 * g + q4) * (NHD * 32) + cp * 32 + p4 * 8;
            const bf16x4 t0 = __builtin_amdgcn_ds_read_tr16_b64_v4i16(
                (__attribute__((address_space(3))) bf16x4*)(hp));
            const bf16x4 t1 = __builtin_amdgcn_ds_read_tr16_b64_v4i16(
                (__attribute__((address_space(3))) bf16x4*)(hp + 4 * (NHD * 32)));
            const bf16x8 wf = __builtin_shufflevector(t0, t1, 0, 1, 2, 3, 4, 5, 6, 7);
            acc[0][bb] = __builtin_amdgcn_mfma_f32_16x16x32_bf16(wf, xf[0][kc * 2 + ks], acc[0][bb], 0, 0, 0);
            acc[1][bb] = __builtin_amdgcn_mfma_f32_16x16x32_bf16(wf, xf[1][kc * 2 + ks], acc[1][bb], 0, 0, 0);
          }
        }
      }
    }
  }

#pragma unroll
  for (int bb = 0; bb < NB0; ++bb) {
    if (wn == 1 && bb == 5) continue;
#pragma unroll
    for (int half = 0; half < 2; ++half) {
      if (half != wn) continue;
      const int b = half * NB0 + bb;
      if (b >= 11) continue;
      if (b < NAB) {
        const int n = c0 + 16 * b + li;
        const bool isb = n >= 512;
        const float sg = isb ? -1.f : 1.f;
        u32x4 o;
        o[0] = pack2(sg * acc[0][bb][0], sg * acc[0][bb][1]);
        o[1] = pack2(sg * acc[0][bb][2], sg * acc[0][bb][3]);
        o[2] = pack2(sg * acc[1][bb][0], sg * acc[1][bb][1]);
        o[3] = pack2(sg * acc[1][bb][2], sg * acc[1][bb][3]);
        u16* dst = BT + (n & 511) * KTOT + (isb ? 2048 : 0) + m0 + 8 * g;
        *(u32x4*)dst = o;
      } else {
        const int f = c0 + 16 * b - 1024 + 4 * g;
        const u32x2 bv = *(const u32x2*)(b1 + f);
        const float bias[4] = {bf_lo(bv[0]), bf_hi(bv[0]), bf_lo(bv[1]), bf_hi(bv[1])};
#pragma unroll
        for (int mb = 0; mb < 2; ++mb) {
          float r[4];
#pragma unroll
          for (int i = 0; i < 4; ++i) {
            float t = rbf(rbf(acc[mb][bb][i]) + bias[i]);
            t = t > 0.f ? t : 0.f;
            r[i] = t * t;
          }
          u32x2 o;
          o[0] = pack2(r[0], r[1]);
          o[1] = pack2(r[2], r[3]);
          const int row = m0 + 8 * (li >> 2) + 4 * mb + (li & 3);
          *(u32x2*)(H + row * FF + f) = o;
        }
      }
    }
  }
}

__global__ __launch_bounds__(512, 2) void k1(const u16* __restrict__ x, const u16* __restrict__ Wr,
                                             const u16* __restrict__ Wi, const u16* __restrict__ w1,
                                             const u16* __restrict__ b1, const u16* __restrict__ w2,
                                             u16* __restrict__ BT, u16* __restrict__ H,
                                             unsigned* __restrict__ cnt) {
  extern __shared__ __attribute__((aligned(16))) char smem[];
  const int bid = blockIdx.x;
  const int xcd = bid & 7, idx = bid >> 3;
  const int mt = (xcd & 1) * 8 + (idx >> 2);
  const int nt = (xcd >> 1) * 4 + (idx & 3);
  const int tid = threadIdx.x;

  u32x4 w2v = (u32x4){0u, 0u, 0u, 0u};
  const int tf0 = (bid >> 3) * 64, te0 = (bid & 7) * 64;
  if (bid < 224) w2v = *(const u32x4*)(w2 + (tf0 + (tid >> 3)) * DM + te0 + (tid & 7) * 8);
  if (bid == 0 && tid < 64) cnt[tid * 64] = 0u;

  const int wn = __builtin_amdgcn_readfirstlane(tid >> 8);
  if (wn == 0) {
    if (nt <= 4) k1_body<11, 0>(x, Wr, Wi, w1, b1, BT, H, smem, mt, nt);
    else if (nt == 5) k1_body<9, 0>(x, Wr, Wi, w1, b1, BT, H, smem, mt, nt);
    else k1_body<0, 0>(x, Wr, Wi, w1, b1, BT, H, smem, mt, nt);
  } else {
    if (nt <= 4) k1_body<11, 1>(x, Wr, Wi, w1, b1, BT, H, smem, mt, nt);
    else if (nt == 5) k1_body<9, 1>(x, Wr, Wi, w1, b1, BT, H, smem, mt, nt);
    else k1_body<0, 1>(x, Wr, Wi, w1, b1, BT, H, smem, mt, nt);
  }

  if (bid < 224) {
    __syncthreads();
    u16* T = (u16*)smem;
    const int f = tid >> 3, e8 = (tid & 7) * 8;
#pragma unroll
    for (int q = 0; q < 4; ++q) {
      T[(e8 + 2 * q) * 72 + f] = (u16)(w2v[q] & 0xffffu);
      T[(e8 + 2 * q + 1) * 72 + f] = (u16)(w2v[q] >> 16);
    }
    __syncthreads();
    const int e = tid >> 3, f8 = (tid & 7) * 8;
    const u32x4 v = *(const u32x4*)(T + e * 72 + f8);
    *(u32x4*)(BT + (te0 + e) * KTOT + 4096 + tf0 + f8) = v;
  }
}

#define K2_NBUF 4
#define K2_TILE 32768
#define K2_LDS (K2_NBUF * K2_TILE)
#define K2_NT 23

template <int SK>
__device__ __forceinline__ void k2_epi(f32x4 (&acc)[4][4], float* __restrict__ slabs, unsigned* __restrict__ cnt,
                                       const u16* __restrict__ x, const u16* __restrict__ b2,
                                       u16* __restrict__ out, int tile, int m0, int n0, int w) {
  const int tid = threadIdx.x, lane = tid & 63;
  const int wm = w >> 1, wn = w & 1;
  const int li = lane & 15, g = lane >> 4;
  const __amdgpu_buffer_rsrc_t rs = __builtin_amdgcn_make_buffer_rsrc((void*)slabs, 0, 64 * 16 * 16384, 0x00020000);
#pragma unroll
  for (int d = 0; d < 4; ++d) {
    if (d == SK) continue;
    const int base = ((tile * 4 + d) * 4 + SK) * 16384 + w * 4096 + lane * 16;
#pragma unroll
    for (int j = 0; j < 4; ++j)
      __builtin_amdgcn_raw_buffer_store_b128(__builtin_bit_cast(u32x4, acc[d][j]), rs, base + j * 1024, 0, 16);
  }
  WAIT_V(0);
  __syncthreads();
  if (tid == 0) {
    unsigned* c = cnt + tile * 64;
    __hip_atomic_fetch_add(c, 1u, __ATOMIC_RELAXED, __HIP_MEMORY_SCOPE_AGENT);
    unsigned spins = 0;
    while (__hip_atomic_load(c, __ATOMIC_RELAXED, __HIP_MEMORY_SCOPE_AGENT) < 4u) {
      __builtin_amdgcn_s_sleep(2);
      if (++spins > (1u << 24)) break;
    }
  }
  __syncthreads();
  f32x4 part[4][4];
#pragma unroll
  for (int s = 0; s < 4; ++s) {
    const int base = ((tile * 4 + SK) * 4 + s) * 16384 + w * 4096 + lane * 16;
#pragma unroll
    for (int j = 0; j < 4; ++j) {
      if (s != SK) part[s][j] = __builtin_bit_cast(f32x4, __builtin_amdgcn_raw_buffer_load_b128(rs, base + j * 1024, 0, 16));
      else part[s][j] = acc[SK][j];
    }
  }
  const int row = m0 + wm * 64 + SK * 16 + li;
#pragma unroll
  for (int j = 0; j < 4; ++j) {
    const f32x4 sum = (part[0][j] + part[1][j]) + (part[2][j] + part[3][j]);
    const int col = n0 + wn * 64 + j * 16 + 4 * g;
    const u32x2 xv = *(const u32x2*)(x + row * DM + col);
    const u32x2 bv = *(const u32x2*)(b2 + col);
    u32x2 o;
    o[0] = pack2(bf_lo(xv[0]) + sum[0] + bf_lo(bv[0]), bf_hi(xv[0]) + sum[1] + bf_hi(bv[0]));
    o[1] = pack2(bf_lo(xv[1]) + sum[2] + bf_lo(bv[1]), bf_hi(xv[1]) + sum[3] + bf_hi(bv[1]));
    *(u32x2*)(out + row * DM + col) = o;
  }
}

__global__ __launch_bounds__(256, 1) void k2(const u16* __restrict__ Cr, const u16* __restrict__ Ci,
                                             const u16* __restrict__ H, const u16* __restrict__ BT,
                                             const u16* __restrict__ x, const u16* __restrict__ b2,
                                             u16* __restrict__ out, float* __restrict__ slabs,
                                             unsigned* __restrict__ cnt) {
  extern __shared__ __attribute__((aligned(16))) char smem[];
  const int bid = blockIdx.x;
  const int xcd = bid & 7, idx = bid >> 3;
  const int sk = xcd >> 1;
  const int mt = (xcd & 1) * 8 + (idx >> 2);
  const int nt = idx & 3;
  const int tid = threadIdx.x, lane = tid & 63;
  const int w = __builtin_amdgcn_readfirstlane(tid >> 6);
  const int wm = w >> 1, wn = w & 1;
  const int li = lane & 15, g = lane >> 4;
  const int m0 = mt * 128, n0 = nt * 128;

  const int row0 = 32 * w + (lane >> 3);
  const int chE = ((lane & 7) ^ (lane >> 4)) * 8, chO = chE ^ 32;
  const int kt0 = sk * K2_NT;
  const u16* bsrc = BT + (n0 + row0) * KTOT;
  auto issue = [&](int t) {
    const int kt = kt0 + t;
    char* dst = smem + (t % K2_NBUF) * K2_TILE + w * 4096;
    const u16* ap = (kt < 32) ? (Cr + kt * 64) : (kt < 64) ? (Ci + (kt - 32) * 64) : (H + (kt - 64) * 64);
    const int pitch = (kt < 64) ? 2048 : FF;
    const u16* asrc = ap + (m0 + row0) * pitch;
#pragma unroll
    for (int j = 0; j < 4; ++j) glds16(asrc + 8 * j * pitch + ((j & 1) ? chO : chE), dst + j * 1024);
    const u16* bp = bsrc + kt * 64;
#pragma unroll
    for (int j = 0; j < 4; ++j) glds16(bp + 8 * j * KTOT + ((j & 1) ? chO : chE), dst + 16384 + j * 1024);
  };

  f32x4 acc[4][4];
#pragma unroll
  for (int i = 0; i < 4; ++i)
#pragma unroll
    for (int j = 0; j < 4; ++j) acc[i][j] = (f32x4){0.f, 0.f, 0.f, 0.f};

  const int rdA0 = (wm * 64 + li) * 128 + ((0 + g) ^ (li >> 1)) * 16;
  const int rdA1 = (wm * 64 + li) * 128 + ((4 + g) ^ (li >> 1)) * 16;
  const int rdB0 = 16384 + (wn * 64 + li) * 128 + ((0 + g) ^ (li >> 1)) * 16;
  const int rdB1 = 16384 + (wn * 64 + li) * 128 + ((4 + g) ^ (li >> 1)) * 16;

  bf16x8 a0[4], b0[4], a1[4], b1f[4];
  issue(0);
  issue(1);
  issue(2);
  wait_vm<16>();
  BAR();
#pragma unroll
  for (int i = 0; i < 4; ++i) a0[i] = *(const bf16x8*)(smem + rdA0 + i * 2048);
#pragma unroll
  for (int i = 0; i < 4; ++i) b0[i] = *(const bf16x8*)(smem + rdB0 + i * 2048);

  for (int t = 0; t < K2_NT; ++t) {
    const char* buf = smem + (t % K2_NBUF) * K2_TILE;
#pragma unroll
    for (int i = 0; i < 4; ++i) a1[i] = *(const bf16x8*)(buf + rdA1 + i * 2048);
#pragma unroll
    for (int i = 0; i < 4; ++i) b1f[i] = *(const bf16x8*)(buf + rdB1 + i * 2048);
#pragma unroll
    for (int i = 0; i < 4; ++i)
#pragma unroll
      for (int j = 0; j < 4; ++j)
        acc[i][j] = __builtin_amdgcn_mfma_f32_16x16x32_bf16(b0[j], a0[i], acc[i][j], 0, 0, 0);
    if (t + 1 < K2_NT) {
      if (t + 2 < K2_NT) wait_vm<8>(); else wait_vm<0>();
      BAR();
      if (t + 3 < K2_NT) issue(t + 3);
      const char* nbuf = smem + ((t + 1) % K2_NBUF) * K2_TILE;
#pragma unroll
      for (int i = 0; i < 4; ++i) a0[i] = *(const bf16x8*)(nbuf + rdA0 + i * 2048);
#pragma unroll
      for (int i = 0; i < 4; ++i) b0[i] = *(const bf16x8*)(nbuf + rdB0 + i * 2048);
    }
#pragma unroll
    for (int i = 0; i < 4; ++i)
#pragma unroll
      for (int j = 0; j < 4; ++j)
        acc[i][j] = __builtin_amdgcn_mfma_f32_16x16x32_bf16(b1f[j], a1[i], acc[i][j], 0, 0, 0);
  }

  const int tile = mt * 4 + nt;
  if (sk == 0) k2_epi<0>(acc, slabs, cnt, x, b2, out, tile, m0, n0, w);
  else if (sk == 1) k2_epi<1>(acc, slabs, cnt, x, b2, out, tile, m0, n0, w);
  else if (sk == 2) k2_epi<2>(acc, slabs, cnt, x, b2, out, tile, m0, n0, w);
  else k2_epi<3>(acc, slabs, cnt, x, b2, out, tile, m0, n0, w);
}

extern "C" void kernel_launch(void* const* d_in, const int* in_sizes, int n_in, void* d_out, int out_size,
                              void* d_ws, size_t ws_size, hipStream_t stream) {
  const u16* x = (const u16*)d_in[0];
  const u16* Cr = (const u16*)d_in[1];
  const u16* Ci = (const u16*)d_in[2];
  const u16* Wr = (const u16*)d_in[3];
  const u16* Wi = (const u16*)d_in[4];
  const u16* w1 = (const u16*)d_in[6];
  const u16* b1 = (const u16*)d_in[7];
  const u16* w2 = (const u16*)d_in[8];
  const u16* b2 = (const u16*)d_in[9];
  u16* out = (u16*)d_out;
  char* ws = (char*)d_ws;
  u16* BT = (u16*)ws;
  u16* H = (u16*)(ws + 6029312);
  float* slabs = (float*)(ws + 6029312 + 7340032);
  unsigned* cnt = (unsigned*)(ws + 6029312 + 7340032 + 16777216);

  (void)hipFuncSetAttribute((const void*)k1, hipFuncAttributeMaxDynamicSharedMemorySize, K1_LDS);
  (void)hipFuncSetAttribute((const void*)k2, hipFuncAttributeMaxDynamicSharedMemorySize, K2_LDS);
  k1<<<256, 512, K1_LDS, stream>>>(x, Wr, Wi, w1, b1, w2, BT, H, cnt);
  k2<<<256, 256, K2_LDS, stream>>>(Cr, Ci, H, BT, x, b2, out, slabs, cnt);
}
```

```cpp
#include <hip/hip_runtime.h>
#include <hip/hip_bf16.h>
#include <stdint.h>

typedef unsigned short u16;
typedef __attribute__((ext_vector_type(8))) short bf16x8;
typedef __attribute__((ext_vector_type(4))) short bf16x4;
typedef __attribute__((ext_vector_type(2))) short bf16x2;
typedef __attribute__((ext_vector_type(4))) float f32x4;
typedef __attribute__((ext_vector_type(4))) unsigned int u32x4;
typedef __attribute__((ext_vector_type(2))) unsigned int u32x2;

#define SEQ 2048
#define DM 512
#define FF 1792
#define KTOT 5888

#define STAMP(i)
#define STAMP2(i)
#define RSTAMP(k, i)
#define WAIT_V(n) asm volatile("s_waitcnt vmcnt(" #n ")" ::: "memory")
#define WAIT_L0() asm volatile("s_waitcnt lgkmcnt(0)" ::: "memory")
#define BAR() asm volatile("s_barrier" ::: "memory")

__device__ __forceinline__ float bf2f(u16 v) { return __uint_as_float(((unsigned)v) << 16); }
__device__ __forceinline__ float bf_lo(unsigned v) { return __uint_as_float(v << 16); }
__device__ __forceinline__ float bf_hi(unsigned v) { return __uint_as_float(v & 0xffff0000u); }
__device__ __forceinline__ unsigned pack2(float lo, float hi) {
  unsigned r;
  asm("v_cvt_pk_bf16_f32 %0, %1, %2" : "=v"(r) : "v"(lo), "v"(hi));
  return r;
}
__device__ __forceinline__ float rbf(float f) { return bf_lo(pack2(f, f)); }
__device__ __forceinline__ float afma(float a, float b, float c) {
  float r;
  asm("v_fma_f32 %0, %1, %2, %3" : "=v"(r) : "v"(a), "v"(b), "v"(c));
  return r;
}
__device__ __forceinline__ float aadd(float a, float b) {
  float r;
  asm("v_add_f32 %0, %1, %2" : "=v"(r) : "v"(a), "v"(b));
  return r;
}
__device__ __forceinline__ float amul(float a, float b) {
  float r;
  asm("v_mul_f32 %0, %1, %2" : "=v"(r) : "v"(a), "v"(b));
  return r;
}

template <int N> __device__ __forceinline__ void wait_vm() {
  asm volatile("s_waitcnt vmcnt(%0)" ::"i"(N) : "memory");
}

__device__ __forceinline__ void glds16(const void* g, void* l) {
  __builtin_amdgcn_global_load_lds((const __attribute__((address_space(1))) unsigned*)g,
                                   (__attribute__((address_space(3))) unsigned*)l, 16, 0, 0);
}

#define K1_PF 8
#define K1_CHUNK 22528
#define K1_XOFF 131072
#define K1_STATS (K1_XOFF + K1_CHUNK)
#define K1_BIAS (K1_STATS + 1024)
#define K1_W2T (K1_BIAS + 512)
#define K1_LDS (K1_W2T + 8192)

__device__ __forceinline__ constexpr int k1_buf(int kc) {
  return (kc == 0 || kc == 6) ? K1_XOFF : (kc == 7 ? 0 : (kc - 1) * K1_CHUNK);
}
typedef __attribute__((ext_vector_type(2))) float f32x2;

template <int NAB, int WN>
__device__ __forceinline__ void k1_body(const u16* __restrict__ x, const u16* __restrict__ Wr,
                                        const u16* __restrict__ Wi, const u16* __restrict__ w1,
                                        const u16* __restrict__ b1, u16* __restrict__ BT,
                                        u16* __restrict__ H, char* smem, int mt, int nt,
                                        const char* __restrict__ pf) {
  constexpr int NH = 11 - NAB;
  constexpr int ROT = (NH == 11) ? 4 : 1;
  constexpr int NHD = (NH == 0) ? 1 : NH;
  const int tid = threadIdx.x;
  const int lane = tid & 63;
  const int w = __builtin_amdgcn_readfirstlane(tid >> 6);
  const int wm = w & 3;
  constexpr int wn = WN;
  const int li = lane & 15, g = lane >> 4;
  const int c0 = nt * 176;
  const int m0 = mt * 128 + wm * 32;

  STAMP(0);
  {
    const u16* xs = x + (mt * 128 + 16 * w) * DM;
#pragma unroll
    for (int r = 0; r < 16; ++r) {
      const int sw = (((r >> 3) | ((w & 1) << 1)) << 2) | (r & 3);
      glds16(xs + r * DM + ((lane ^ sw) * 8), smem + (16 * w + r) * 1024);
    }
  }

  auto issue_n = [&](int kc0, int kc1) {
    int ln = lane;
    asm volatile("" : "+v"(ln));
    const u16* src[3];
    int inc[3], jd[3];
#pragma unroll
    for (int jj = 0; jj < 3; ++jj) {
      int j = w + 8 * jj;
      if (j > 21) j -= 8;
      jd[jj] = j * 1024;
      if (NAB == 11 || (NAB > 0 && j < 2 * NAB)) {
        const int row = 8 * j + (ln >> 3);
        const int ch = (ln & 7) ^ ((row >> 1) & 7);
        const int n = c0 + row;
        const u16* base = (n < 512) ? (Wr + n * DM) : (Wi + (n - 512) * DM);
        src[jj] = base + ch * 8;
        inc[jj] = 64;
      } else {
        const unsigned p16 = (j - 2 * NAB) * 64 + ln;
        const unsigned r = p16 / (2 * NHD);
        const unsigned rem = p16 - r * (2 * NHD);
        const int cp = rem >> 1, hf = rem & 1;
        int c = cp - ROT * ((r >> 3) & 1);
        if (c < 0) c += NH;
        src[jj] = w1 + r * FF + (c0 + 16 * NAB - 1024) + 16 * c + 8 * hf;
        inc[jj] = 64 * FF;
      }
    }
#pragma unroll
    for (int kc = kc0; kc < kc1; ++kc)
#pragma unroll
      for (int jj = 0; jj < 3; ++jj) glds16(src[jj] + kc * inc[jj], smem + k1_buf(kc) + jd[jj]);
  };
  auto issue = [&](int kc) { issue_n(kc, kc + 1); };
  issue(0);
  if (NH > 0 && tid < NH * 4)
    *(u32x2*)(smem + K1_BIAS + tid * 8) = *(const u32x2*)(b1 + (c0 + 16 * NAB - 1024) + 4 * tid);
  wait_vm<0>();
  WAIT_L0();
  BAR();
  STAMP(1);
  bf16x8 xf[2][16];
#pragma unroll
  for (int mb = 0; mb < 2; ++mb) {
    const char* xp = smem + (wm * 32 + 8 * (li >> 2) + 4 * mb + (li & 3)) * 1024;
#pragma unroll
    for (int ks = 0; ks < 16; ++ks)
      xf[mb][ks] = *(const bf16x8*)(xp + (ks >> 2) * 256 + (((4 * (ks & 3) + g) ^ li) * 16));
  }
  WAIT_L0();
  BAR();
  issue_n(1, 6);
  STAMP(2);

  {
    float s0 = 0.f, s1 = 0.f, q0 = 0.f, q1 = 0.f;
#pragma unroll
    for (int ks = 0; ks < 16; ++ks) {
      const u32x4 v = __builtin_bit_cast(u32x4, xf[wn][ks]);
      asm("v_dot2c_f32_bf16 %0, 0x3f803f80, %4\n\tv_dot2c_f32_bf16 %1, 0x3f803f80, %5\n\t"
          "v_dot2c_f32_bf16 %2, %4, %4\n\tv_dot2c_f32_bf16 %3, %5, %5\n\t"
          "v_dot2c_f32_bf16 %0, 0x3f803f80, %6\n\tv_dot2c_f32_bf16 %1, 0x3f803f80, %7\n\t"
          "v_dot2c_f32_bf16 %2, %6, %6\n\tv_dot2c_f32_bf16 %3, %7, %7"
          : "+v"(s0), "+v"(s1), "+v"(q0), "+v"(q1)
          : "v"(v[0]), "v"(v[1]), "v"(v[2]), "v"(v[3]));
    }
    asm volatile("s_nop 3" : "+v"(s0), "+v"(s1), "+v"(q0), "+v"(q1));
    float s = s0 + s1, q = q0 + q1;
    s += __shfl_xor(s, 16);
    q += __shfl_xor(q, 16);
    s += __shfl_xor(s, 32);
    q += __shfl_xor(q, 32);
    const float mean = amul(s, 1.0f / 512.0f);
    float varf = afma(q, 1.0f / 512.0f, -mean * mean);
    if (__any(amul(mean, mean) > amul(8.0f, varf))) {
      float v0 = 0.f, v1 = 0.f;
#pragma unroll
      for (int ks = 0; ks < 16; ++ks) {
        const u32x4 v = __builtin_bit_cast(u32x4, xf[wn][ks]);
#pragma unroll
        for (int qq = 0; qq < 4; ++qq) {
          const float d0 = aadd(bf_lo(v[qq]), -mean), d1 = aadd(bf_hi(v[qq]), -mean);
          v0 = afma(d0, d0, v0);
          v1 = afma(d1, d1, v1);
        }
      }
      float vs = v0 + v1;
      vs += __shfl_xor(vs, 16);
      vs += __shfl_xor(vs, 32);
      varf = vs * (1.0f / 512.0f);
    }
    const float var = rbf(varf);
    const float sd = rbf(sqrtf(rbf(var + 1e-5f)));
    const float rinv = 1.0f / sd;
    const float mb16 = rbf(mean);
    if (g == 0) {
      float2* st = (float2*)(smem + K1_STATS);
      st[wm * 32 + 8 * (li >> 2) + 4 * wn + (li & 3)] = make_float2(rinv, -mb16 * rinv);
    }
  }
  STAMP(3);

  constexpr int NB0 = 6;
  f32x4 acc[2][NB0];
#pragma unroll
  for (int mb = 0; mb < 2; ++mb)
#pragma unroll
    for (int b = 0; b < NB0; ++b) acc[mb][b] = (f32x4){0.f, 0.f, 0.f, 0.f};
  f32x4 cs[NB0];
#pragma unroll
  for (int b = 0; b < NB0; ++b) cs[b] = (f32x4){0.f, 0.f, 0.f, 0.f};
  bf16x8 ones = {0x3f80, 0x3f80, 0x3f80, 0x3f80, 0x3f80, 0x3f80, 0x3f80, 0x3f80};
  asm volatile("" : "+v"(ones));

  const int q4 = li >> 2, p4 = lane & 3;
  constexpr int NBW = (wn == 0) ? 6 : 5;
  bf16x8 wf[NBW];
  const int laneAB0 = li * 128 + ((0 + g) ^ (li >> 1)) * 16, laneAB1 = li * 128 + ((4 + g) ^ (li >> 1)) * 16;
  const int laneH0 = NAB * 2048 + (8 * g + q4) * (NHD * 32) + (g & 1) * ROT * 32 + p4 * 8;
  const int laneH1 = laneH0 - (g & 1) * NH * 32;
  int boff = 0;
  auto setbuf = [&](int kc) {
    boff = k1_buf(kc);
    asm volatile("" : "+s"(boff));
  };
  auto rdw = [&](int step, int bb) {
    const int ks = step & 1;
    const int b = wn * NB0 + bb;
    if (b < NAB) {
      wf[bb] = *(const bf16x8*)(smem + boff + (ks ? laneAB1 : laneAB0) + b * 2048);
    } else {
      const int hb = b - NAB;
      const char* hp = smem + boff + ((hb + ROT >= NH) ? laneH1 : laneH0) + 32 * ks * (NHD * 32) + hb * 32;
      const bf16x4 t0 = __builtin_amdgcn_ds_read_tr16_b64_v4i16((__attribute__((address_space(3))) bf16x4*)(hp));
      const bf16x4 t1 = __builtin_amdgcn_ds_read_tr16_b64_v4i16((__attribute__((address_space(3))) bf16x4*)(hp + 4 * (NHD * 32)));
      wf[bb] = __builtin_shufflevector(t0, t1, 0, 1, 2, 3, 4, 5, 6, 7);
    }
  };
  setbuf(0);
#pragma unroll
  for (int bb = 0; bb < NBW; ++bb) asm volatile("s_nop 1" : "+v"(acc[0][bb]), "+v"(acc[1][bb]), "+v"(cs[bb]));
#pragma unroll
  for (int bb = 0; bb < NBW; ++bb) rdw(0, bb);
#pragma unroll
  for (int step = 0; step < 16; ++step) {
    const int kc = step >> 1;
    if ((step & 1) && kc < 7) {
      if (kc == 0) wait_vm<12>(); else if (kc <= 2) wait_vm<9>(); else if (kc == 3) wait_vm<11>(); else if (kc == 4) wait_vm<10>(); else if (kc == 5) wait_vm<9>(); else wait_vm<8>();
      BAR();
      if (kc == 1) issue(6);
      if (kc == 2) issue(7);
      if (kc >= 2 && kc <= 5) {
        glds16(pf + (2 * kc - 4) * 8192, smem + 5 * K1_CHUNK + w * 1024);
        glds16(pf + (2 * kc - 3) * 8192, smem + 5 * K1_CHUNK + w * 1024);
      }
      setbuf(kc + 1);
      STAMP(4 + kc);
    }
#pragma unroll
    for (int bb = 0; bb < NBW; ++bb) {
      const int b = wn * NB0 + bb;
      if (b < NAB) {
        asm("v_mfma_f32_16x16x32_bf16 %0, %3, %5, %0\n\tv_mfma_f32_16x16x32_bf16 %1, %4, %5, %1\n\t"
            "v_mfma_f32_16x16x32_bf16 %2, %6, %5, %2"
            : "+v"(acc[0][bb]), "+v"(acc[1][bb]), "+v"(cs[bb])
            : "v"(xf[0][step]), "v"(xf[1][step]), "v"(wf[bb]), "v"(ones));
      } else {
        asm("v_mfma_f32_16x16x32_bf16 %0, %5, %3, %0\n\tv_mfma_f32_16x16x32_bf16 %1, %5, %4, %1\n\t"
            "v_mfma_f32_16x16x32_bf16 %2, %5, %6, %2"
            : "+v"(acc[0][bb]), "+v"(acc[1][bb]), "+v"(cs[bb])
            : "v"(xf[0][step]), "v"(xf[1][step]), "v"(wf[bb]), "v"(ones));
      }
      if (step < 15) rdw(step + 1, bb);
      __builtin_amdgcn_sched_barrier(0);
    }
  }

  STAMP(12);
  int tz = threadIdx.x;
  asm volatile("" : "+v"(tz));
  const int eli = tz & 15, eg = (tz >> 4) & 3;
  const float2* stp = (const float2*)(smem + K1_STATS) + wm * 32;
  float2 sta[8];
  float2 sth[2];
  if (NAB > wn * NB0) {
#pragma unroll
    for (int i = 0; i < 8; ++i) sta[i] = stp[8 * eg + i];
  }
  if (NAB < 11 && !(wn == 0 && NAB >= NB0)) {
#pragma unroll
    for (int mb = 0; mb < 2; ++mb) sth[mb] = stp[8 * (eli >> 2) + 4 * mb + (eli & 3)];
  }
#pragma unroll
  for (int bb = 0; bb < NBW; ++bb) {
    const int b = wn * NB0 + bb;
    asm volatile("s_nop 7" : "+v"(acc[0][bb]), "+v"(acc[1][bb]), "+v"(cs[bb]));
    if (b < NAB) {
      const int n = c0 + 16 * b + eli;
      const bool isb = n >= 512;
      const unsigned sgn = isb ? 0x80008000u : 0u;
      u32x4 o;
#pragma unroll
      for (int mb = 0; mb < 2; ++mb) {
        float t0, t1, t2, t3;
        unsigned o0, o1;
        asm("v_mul_f32 %2, %11, %10\n\tv_mul_f32 %3, %13, %10\n\tv_mul_f32 %4, %15, %10\n\tv_mul_f32 %5, %17, %10\n\t"
            "v_fma_f32 %2, %12, %6, %2\n\tv_fma_f32 %3, %14, %7, %3\n\tv_fma_f32 %4, %16, %8, %4\n\tv_fma_f32 %5, %18, %9, %5\n\t"
            "v_cvt_pk_bf16_f32 %0, %2, %3\n\tv_cvt_pk_bf16_f32 %1, %4, %5\n\t"
            "v_xor_b32 %0, %19, %0\n\tv_xor_b32 %1, %19, %1"
            : "=&v"(o0), "=&v"(o1), "=&v"(t0), "=&v"(t1), "=&v"(t2), "=&v"(t3)
            : "v"(acc[mb][bb][0]), "v"(acc[mb][bb][1]), "v"(acc[mb][bb][2]), "v"(acc[mb][bb][3]), "v"(cs[bb][0]),
              "v"(sta[4 * mb + 0].y), "v"(sta[4 * mb + 0].x), "v"(sta[4 * mb + 1].y), "v"(sta[4 * mb + 1].x),
              "v"(sta[4 * mb + 2].y), "v"(sta[4 * mb + 2].x), "v"(sta[4 * mb + 3].y), "v"(sta[4 * mb + 3].x),
              "v"(sgn));
        o[2 * mb] = o0;
        o[2 * mb + 1] = o1;
      }
      u16* dst = BT + (n & 511) * KTOT + (isb ? 2048 : 0) + m0 + 8 * eg;
      *(u32x4*)dst = o;
    } else {
      const int fl = 16 * (b - NAB) + 4 * eg;
      const u32x2 bv = *(const u32x2*)(smem + K1_BIAS + fl * 2);
      const float bias0 = bf_lo(bv[0]), bias1 = bf_hi(bv[0]), bias2 = bf_lo(bv[1]), bias3 = bf_hi(bv[1]);
#pragma unroll
      for (int mb = 0; mb < 2; ++mb) {
        float t0, t1, t2, t3;
        unsigned o0, o1;
        asm("v_fma_f32 %2, %19, %10, %14\n\tv_fma_f32 %3, %19, %11, %15\n\tv_fma_f32 %4, %19, %12, %16\n\tv_fma_f32 %5, %19, %13, %17\n\t"
            "v_fma_f32 %2, %18, %6, %2\n\tv_fma_f32 %3, %18, %7, %3\n\tv_fma_f32 %4, %18, %8, %4\n\tv_fma_f32 %5, %18, %9, %5\n\t"
            "v_max_f32 %2, 0, %2\n\tv_max_f32 %3, 0, %3\n\tv_max_f32 %4, 0, %4\n\tv_max_f32 %5, 0, %5\n\t"
            "v_cvt_pk_bf16_f32 %0, %2, %3\n\tv_cvt_pk_bf16_f32 %1, %4, %5\n\t"
            "v_lshlrev_b32 %2, 16, %0\n\tv_and_b32 %3, 0xffff0000, %0\n\tv_lshlrev_b32 %4, 16, %1\n\tv_and_b32 %5, 0xffff0000, %1\n\t"
            "v_mul_f32 %2, %2, %2\n\tv_mul_f32 %3, %3, %3\n\tv_mul_f32 %4, %4, %4\n\tv_mul_f32 %5, %5, %5\n\t"
            "v_cvt_pk_bf16_f32 %0, %2, %3\n\tv_cvt_pk_bf16_f32 %1, %4, %5"
            : "=&v"(o0), "=&v"(o1), "=&v"(t0), "=&v"(t1), "=&v"(t2), "=&v"(t3)
            : "v"(acc[mb][bb][0]), "v"(acc[mb][bb][1]), "v"(acc[mb][bb][2]), "v"(acc[mb][bb][3]),
              "v"(cs[bb][0]), "v"(cs[bb][1]), "v"(cs[bb][2]), "v"(cs[bb][3]),
              "v"(bias0), "v"(bias1), "v"(bias2), "v"(bias3), "v"(sth[mb].x), "v"(sth[mb].y));
        u32x2 o;
        o[0] = o0;
        o[1] = o1;
        const int row = m0 + 8 * (eli >> 2) + 4 * mb + (eli & 3);
        *(u32x2*)(H + row * FF + (c0 + 16 * NAB - 1024) + fl) = o;
      }
    }
  }
}

__global__ __launch_bounds__(512, 2) void k1(const u16* __restrict__ x, const u16* __restrict__ Wr,
                                             const u16* __restrict__ Wi, const u16* __restrict__ w1,
                                             const u16* __restrict__ b1, const u16* __restrict__ w2,
                                             u16* __restrict__ BT, u16* __restrict__ H,
                                             unsigned* __restrict__ cnt, const u16* __restrict__ Cr,
                                             const u16* __restrict__ Ci) {
  extern __shared__ __attribute__((aligned(16))) char smem[];
  const int bid = blockIdx.x;
  const int xcd = bid & 7, idx = bid >> 3;
  const int mt = (xcd & 1) * 8 + (idx >> 2);
  const int nt = (xcd >> 1) * 4 + (idx & 3);
  const int tid = threadIdx.x;

  RSTAMP(0, 30);
  const int tf0 = (bid >> 3) * 64, te0 = (bid & 7) * 64;
  if (bid < 224) {
    const u32x4 w2v = *(const u32x4*)(w2 + (tf0 + (tid >> 3)) * DM + te0 + (tid & 7) * 8);
    u16* T = (u16*)(smem + K1_W2T);
    const int f = tid >> 3, e8 = (tid & 7) * 8;
#pragma unroll
    for (int q = 0; q < 4; ++q) {
      T[(e8 + 2 * q) * 64 + f] = (u16)(w2v[q] & 0xffffu);
      T[(e8 + 2 * q + 1) * 64 + f] = (u16)(w2v[q] >> 16);
    }
  }
  if (bid == 0 && tid < 64) cnt[tid * 64] = 0u;

  const char* pf = (const char*)((bid < 128) ? Cr : Ci) + (size_t)(bid & 127) * 65536 + tid * 16;
  const int wn = __builtin_amdgcn_readfirstlane(tid >> 8);
  if (wn == 0) {
    if (nt <= 4) k1_body<11, 0>(x, Wr, Wi, w1, b1, BT, H, smem, mt, nt, pf);
    else if (nt == 5) k1_body<9, 0>(x, Wr, Wi, w1, b1, BT, H, smem, mt, nt, pf);
    else k1_body<0, 0>(x, Wr, Wi, w1, b1, BT, H, smem, mt, nt, pf);
  } else {
    if (nt <= 4) k1_body<11, 1>(x, Wr, Wi, w1, b1, BT, H, smem, mt, nt, pf);
    else if (nt == 5) k1_body<9, 1>(x, Wr, Wi, w1, b1, BT, H, smem, mt, nt, pf);
    else k1_body<0, 1>(x, Wr, Wi, w1, b1, BT, H, smem, mt, nt, pf);
  }

  STAMP(13);
  if (bid < 224) {
    const int e = tid >> 3, f8 = (tid & 7) * 8;
    const u32x4 v = *(const u32x4*)(smem + K1_W2T + (e * 64 + f8) * 2);
    *(u32x4*)(BT + (te0 + e) * KTOT + 4096 + tf0 + f8) = v;
  }
  wait_vm<0>();
  RSTAMP(0, 31);
}

#define K2_NBUF 4
#define K2_TILE 32768
#define K2_LDS (K2_NBUF * K2_TILE)
#define K2_NT 23

template <int SK>
__device__ __forceinline__ void k2_epi(f32x4 (&r)[2][4], float* __restrict__ slabs, unsigned* __restrict__ cnt,
                                       const u16* __restrict__ x, const u16* __restrict__ b2,
                                       u16* __restrict__ out, int tile, int row0, int col0, int w) {
  const int tid = threadIdx.x, lane = tid & 63;
  const int li = lane & 15, g = lane >> 4;
  const __amdgpu_buffer_rsrc_t rs = __builtin_amdgcn_make_buffer_rsrc((void*)slabs, 0, 64 * 16 * 8192, 0x00020000);
#pragma unroll
  for (int d = 0; d < 4; ++d) {
    if (d == SK) continue;
    const f32x4 p0 = r[d >> 1][2 * (d & 1)], p1 = r[d >> 1][2 * (d & 1) + 1];
    u32x4 o;
    o[0] = pack2(p0[0], p0[1]);
    o[1] = pack2(p0[2], p0[3]);
    o[2] = pack2(p1[0], p1[1]);
    o[3] = pack2(p1[2], p1[3]);
    __builtin_amdgcn_raw_buffer_store_b128(o, rs, ((tile * 4 + d) * 4 + SK) * 8192 + w * 1024 + lane * 16, 0, 16);
  }
  const int row = row0 + (SK >> 1) * 16 + li;
  const int colb = col0 + (SK & 1) * 32 + 4 * g;
  u32x2 xv[2], bv[2];
#pragma unroll
  for (int j = 0; j < 2; ++j) {
    xv[j] = *(const u32x2*)(x + row * DM + colb + j * 16);
    bv[j] = *(const u32x2*)(b2 + colb + j * 16);
  }
  WAIT_V(0);
  STAMP2(6);
  __syncthreads();
  if (tid == 0) {
    unsigned* c = cnt + tile * 64;
    __hip_atomic_fetch_add(c, 1u, __ATOMIC_RELAXED, __HIP_MEMORY_SCOPE_AGENT);
    unsigned spins = 0;
    while (__hip_atomic_load(c, __ATOMIC_RELAXED, __HIP_MEMORY_SCOPE_AGENT) < 4u) {
      __builtin_amdgcn_s_sleep(1);
      if (++spins > (1u << 24)) break;
    }
  }
  __syncthreads();
  STAMP2(7);
  u32x4 part[4];
#pragma unroll
  for (int s = 0; s < 4; ++s)
    if (s != SK) part[s] = __builtin_amdgcn_raw_buffer_load_b128(rs, ((tile * 4 + SK) * 4 + s) * 8192 + w * 1024 + lane * 16, 0, 16);
#pragma unroll
  for (int j = 0; j < 2; ++j) {
    float sum[4];
#pragma unroll
    for (int i = 0; i < 4; ++i) {
      float t = r[SK >> 1][2 * (SK & 1) + j][i];
#pragma unroll
      for (int s = 0; s < 4; ++s)
        if (s != SK) t = aadd(t, (i & 1) ? bf_hi(part[s][2 * j + (i >> 1)]) : bf_lo(part[s][2 * j + (i >> 1)]));
      sum[i] = t;
    }
    u32x2 o;
    o[0] = pack2(aadd(aadd(bf_lo(xv[j][0]), sum[0]), bf_lo(bv[j][0])), aadd(aadd(bf_hi(xv[j][0]), sum[1]), bf_hi(bv[j][0])));
    o[1] = pack2(aadd(aadd(bf_lo(xv[j][1]), sum[2]), bf_lo(bv[j][1])), aadd(aadd(bf_hi(xv[j][1]), sum[3]), bf_hi(bv[j][1])));
    *(u32x2*)(out + row * DM + colb + j * 16) = o;
  }
  STAMP2(8);
}

__global__ __launch_bounds__(512, 2) void k2(const u16* __restrict__ Cr, const u16* __restrict__ Ci,
                                             const u16* __restrict__ H, const u16* __restrict__ BT,
                                             const u16* __restrict__ x, const u16* __restrict__ b2,
                                             u16* __restrict__ out, float* __restrict__ slabs,
                                             unsigned* __restrict__ cnt) {
  extern __shared__ __attribute__((aligned(16))) char smem[];
  const int bid = blockIdx.x;
  const int xcd = bid & 7, idx = bid >> 3;
  const int sk = xcd >> 1;
  const int mt = (xcd & 1) * 8 + (idx >> 2);
  const int nt = idx & 3;
  const int tid = threadIdx.x, lane = tid & 63;
  const int w = __builtin_amdgcn_readfirstlane(tid >> 6);
  const int ksel = w >> 2, wm = (w >> 1) & 1, wn = w & 1;
  const int li = lane & 15, g = lane >> 4;
  const int m0 = mt * 128, n0 = nt * 128;

  RSTAMP(1, 30);
  const int row0 = 16 * w + (lane >> 3);
  const int chE = ((lane & 7) ^ (lane >> 4)) * 8, chO = chE ^ 32;
  const int kt0 = sk * K2_NT;
  const u16* bsrc = BT + (n0 + row0) * KTOT;
  auto issue = [&](int t) {
    const int kt = kt0 + t;
    char* dst = smem + (t % K2_NBUF) * K2_TILE + w * 2048;
    const u16* ap = (kt < 32) ? (Cr + kt * 64) : (kt < 64) ? (Ci + (kt - 32) * 64) : (H + (kt - 64) * 64);
    const int pitch = (kt < 64) ? 2048 : FF;
    const u16* asrc = ap + (m0 + row0) * pitch;
    glds16(asrc + chE, dst);
    glds16(asrc + 8 * pitch + chO, dst + 1024);
    const u16* bp = bsrc + kt * 64;
    glds16(bp + chE, dst + 16384);
    glds16(bp + 8 * KTOT + chO, dst + 16384 + 1024);
  };

  f32x4 acc[4][4];
#pragma unroll
  for (int i = 0; i < 4; ++i)
#pragma unroll
    for (int j = 0; j < 4; ++j) acc[i][j] = (f32x4){0.f, 0.f, 0.f, 0.f};

  const int rdA = (wm * 64 + li) * 128 + ((4 * ksel + g) ^ (li >> 1)) * 16;
  const int rdB = 16384 + (wn * 64 + li) * 128 + ((4 * ksel + g) ^ (li >> 1)) * 16;

  bf16x8 af[4], bfr[4];
  auto rd = [&](int t) {
    const char* buf = smem + (t % K2_NBUF) * K2_TILE;
#pragma unroll
    for (int i = 0; i < 4; ++i) bfr[i] = *(const bf16x8*)(buf + rdB + i * 2048);
#pragma unroll
    for (int i = 0; i < 4; ++i) af[i] = *(const bf16x8*)(buf + rdA + i * 2048);
  };
  auto mm = [&]() {
#pragma unroll
    for (int i = 0; i < 4; ++i)
#pragma unroll
      for (int j = 0; j < 4; ++j)
        acc[i][j] = __builtin_amdgcn_mfma_f32_16x16x32_bf16(bfr[j], af[i], acc[i][j], 0, 0, 0);
  };
  STAMP2(0);
  issue(0);
  issue(1);
  issue(2);
  if (w < 4) {
    for (int t = 0; t < K2_NT; ++t) {
      if (t + 2 < K2_NT) wait_vm<8>(); else if (t + 1 < K2_NT) wait_vm<4>(); else wait_vm<0>();
      WAIT_L0();
      BAR();
      if (t == 0) STAMP2(1); if (t == 4) STAMP2(2); if (t == 12) STAMP2(3); if (t == 20) STAMP2(4);
      if (t + 3 < K2_NT) issue(t + 3);
      rd(t);
      __builtin_amdgcn_sched_barrier(0);
      mm();
      __builtin_amdgcn_sched_barrier(0);
    }
  } else {
    wait_vm<8>();
    BAR();
    STAMP2(1);
    issue(3);
    rd(0);
    for (int t = 1; t < K2_NT; ++t) {
      if (t + 2 < K2_NT) wait_vm<8>(); else if (t + 1 < K2_NT) wait_vm<4>(); else wait_vm<0>();
      WAIT_L0();
      BAR();
      if (t == 4) STAMP2(2); if (t == 12) STAMP2(3); if (t == 20) STAMP2(4);
      mm();
      __builtin_amdgcn_sched_barrier(0);
      if (t + 3 < K2_NT) issue(t + 3);
      rd(t);
      __builtin_amdgcn_sched_barrier(0);
    }
    mm();
  }
  STAMP2(5);
  f32x4 r[2][4];
  WAIT_L0();
  BAR();
  {
    char* mine = smem + w * 8192 + lane * 16;
    const char* theirs = smem + (w ^ 4) * 8192 + lane * 16;
    if (ksel == 0) {
#pragma unroll
      for (int i = 0; i < 2; ++i)
#pragma unroll
        for (int j = 0; j < 4; ++j) { *(f32x4*)(mine + (i * 4 + j) * 1024) = acc[2 + i][j]; r[i][j] = acc[i][j]; }
    } else {
#pragma unroll
      for (int i = 0; i < 2; ++i)
#pragma unroll
        for (int j = 0; j < 4; ++j) { *(f32x4*)(mine + (i * 4 + j) * 1024) = acc[i][j]; r[i][j] = acc[2 + i][j]; }
    }
    WAIT_L0();
    BAR();
    f32x4 o[2][4];
#pragma unroll
    for (int i = 0; i < 2; ++i)
#pragma unroll
      for (int j = 0; j < 4; ++j) o[i][j] = *(const f32x4*)(theirs + (i * 4 + j) * 1024);
#pragma unroll
    for (int i = 0; i < 2; ++i)
#pragma unroll
      for (int j = 0; j < 4; ++j)
#pragma unroll
        for (int q = 0; q < 4; ++q) r[i][j][q] = aadd(r[i][j][q], o[i][j][q]);
  }
  const int tile = mt * 4 + nt;
  const int rw0 = m0 + wm * 64 + ksel * 32, cl0 = n0 + wn * 64;
  if (sk == 0) k2_epi<0>(r, slabs, cnt, x, b2, out, tile, rw0, cl0, w);
  else if (sk == 1) k2_epi<1>(r, slabs, cnt, x, b2, out, tile, rw0, cl0, w);
  else if (sk == 2) k2_epi<2>(r, slabs, cnt, x, b2, out, tile, rw0, cl0, w);
  else k2_epi<3>(r, slabs, cnt, x, b2, out, tile, rw0, cl0, w);
  RSTAMP(1, 31);
}

extern "C" void kernel_launch(void* const* d_in, const int* in_sizes, int n_in, void* d_out, int out_size,
                              void* d_ws, size_t ws_size, hipStream_t stream) {
  const u16* x = (const u16*)d_in[0];
  const u16* Cr = (const u16*)d_in[1];
  const u16* Ci = (const u16*)d_in[2];
  const u16* Wr = (const u16*)d_in[3];
  const u16* Wi = (const u16*)d_in[4];
  const u16* w1 = (const u16*)d_in[6];
  const u16* b1 = (const u16*)d_in[7];
  const u16* w2 = (const u16*)d_in[8];
  const u16* b2 = (const u16*)d_in[9];
  u16* out = (u16*)d_out;
  char* ws = (char*)d_ws;
  u16* BT = (u16*)ws;
  u16* H = (u16*)(ws + 6029312);
  float* slabs = (float*)(ws + 6029312 + 7340032);
  unsigned* cnt = (unsigned*)(ws + 6029312 + 7340032 + 16777216);

  (void)hipFuncSetAttribute((const void*)k1, hipFuncAttributeMaxDynamicSharedMemorySize, K1_LDS);
  (void)hipFuncSetAttribute((const void*)k2, hipFuncAttributeMaxDynamicSharedMemorySize, K2_LDS);
  k1<<<256, 512, K1_LDS, stream>>>(x, Wr, Wi, w1, b1, w2, BT, H, cnt, Cr, Ci);
  k2<<<256, 512, K2_LDS, stream>>>(Cr, Ci, H, BT, x, b2, out, slabs, cnt);
}
```

```cpp
#include <hip/hip_runtime.h>
#include <hip/hip_bf16.h>
#include <stdint.h>

typedef unsigned short u16;
typedef __attribute__((ext_vector_type(8))) short bf16x8;
typedef __attribute__((ext_vector_type(4))) short bf16x4;
typedef __attribute__((ext_vector_type(2))) short bf16x2;
typedef __attribute__((ext_vector_type(4))) float f32x4;
typedef __attribute__((ext_vector_type(4))) unsigned int u32x4;
typedef __attribute__((ext_vector_type(2))) unsigned int u32x2;

#define SEQ 2048
#define DM 512
#define FF 1792
#define KTOT 5888

#define STAMP(i)
#define STAMP2(i)
#define RSTAMP(k, i)
#define WAIT_V(n) asm volatile("s_waitcnt vmcnt(" #n ")" ::: "memory")
#define WAIT_L0() asm volatile("s_waitcnt lgkmcnt(0)" ::: "memory")
#define BAR() asm volatile("s_barrier" ::: "memory")

__device__ __forceinline__ float bf2f(u16 v) { return __uint_as_float(((unsigned)v) << 16); }
__device__ __forceinline__ float bf_lo(unsigned v) { return __uint_as_float(v << 16); }
__device__ __forceinline__ float bf_hi(unsigned v) { return __uint_as_float(v & 0xffff0000u); }
__device__ __forceinline__ unsigned pack2(float lo, float hi) {
  unsigned r;
  asm("v_cvt_pk_bf16_f32 %0, %1, %2" : "=v"(r) : "v"(lo), "v"(hi));
  return r;
}
__device__ __forceinline__ float rbf(float f) { return bf_lo(pack2(f, f)); }
__device__ __forceinline__ float afma(float a, float b, float c) {
  float r;
  asm("v_fma_f32 %0, %1, %2, %3" : "=v"(r) : "v"(a), "v"(b), "v"(c));
  return r;
}
__device__ __forceinline__ float aadd(float a, float b) {
  float r;
  asm("v_add_f32 %0, %1, %2" : "=v"(r) : "v"(a), "v"(b));
  return r;
}
__device__ __forceinline__ float amul(float a, float b) {
  float r;
  asm("v_mul_f32 %0, %1, %2" : "=v"(r) : "v"(a), "v"(b));
  return r;
}

template <int N> __device__ __forceinline__ void wait_vm() {
  asm volatile("s_waitcnt vmcnt(%0)" ::"i"(N) : "memory");
}

__device__ __forceinline__ void glds16(const void* g, void* l) {
  __builtin_amdgcn_global_load_lds((const __attribute__((address_space(1))) unsigned*)g,
                                   (__attribute__((address_space(3))) unsigned*)l, 16, 0, 0);
}

#define PF_KC 6
#define K1_CHUNK 38912
#define K1_NBUF 4
#define K1_STATS (K1_NBUF * K1_CHUNK)
#define K1_BIAS (K1_STATS + 1024)
#define K1_LDS (K1_BIAS + 512)

template <int NAB, int WN>
__device__ __forceinline__ void k1_body(const u16* __restrict__ x, const u16* __restrict__ Wr,
                                        const u16* __restrict__ Wi, const u16* __restrict__ w1,
                                        const u16* __restrict__ b1, u16* __restrict__ BT,
                                        u16* __restrict__ H, char* smem, int mt, int nt,
                                        const char* pf) {
  constexpr int NH = 11 - NAB;
  constexpr int ROT = (NH == 11) ? 4 : 1;
  constexpr int NHD = (NH == 0) ? 1 : NH;
  constexpr int NB0 = 6;
  constexpr int wn = WN;
  constexpr int NBW = (wn == 0) ? 6 : 5;
  const int tid = threadIdx.x;
  const int lane = tid & 63;
  const int w = __builtin_amdgcn_readfirstlane(tid >> 6);
  const int wm = w & 3;
  const int li = lane & 15, g = lane >> 4;
  const int c0 = nt * 176;
  const int m0 = mt * 128 + wm * 32;

  STAMP(0);
  const u16* src[5];
  int inc[5], jd[5];
#pragma unroll
  for (int jj = 0; jj < 2; ++jj) {
    const int row = 16 * w + 8 * jj + (lane >> 3);
    const int sw = (((row >> 3) & 3) << 1) | ((row >> 1) & 1);
    src[jj] = x + (mt * 128 + row) * DM + ((lane & 7) ^ sw) * 8;
    inc[jj] = 64;
    jd[jj] = (2 * w + jj) * 1024;
  }
#pragma unroll
  for (int jj = 0; jj < 3; ++jj) {
    int j = w + 8 * jj;
    if (j > 21) j -= 8;
    jd[2 + jj] = 16384 + j * 1024;
    if (NAB == 11 || (NAB > 0 && j < 2 * NAB)) {
      const int row = 8 * j + (lane >> 3);
      const int ch = (lane & 7) ^ ((row >> 1) & 7);
      const int n = c0 + row;
      const u16* base = (n < 512) ? (Wr + n * DM) : (Wi + (n - 512) * DM);
      src[2 + jj] = base + ch * 8;
      inc[2 + jj] = 64;
    } else {
      const unsigned p16 = (j - 2 * NAB) * 64 + lane;
      const unsigned r = p16 / (2 * NHD);
      const unsigned rem = p16 - r * (2 * NHD);
      const int cp = rem >> 1, hf = rem & 1;
      int c = cp - ROT * ((r >> 3) & 1);
      if (c < 0) c += NH;
      src[2 + jj] = w1 + r * FF + (c0 + 16 * NAB - 1024) + 16 * c + 8 * hf;
      inc[2 + jj] = 64 * FF;
    }
  }
  auto issue = [&](int kc) {
#pragma unroll
    for (int jj = 0; jj < 5; ++jj) glds16(src[jj] + kc * inc[jj], smem + (kc % K1_NBUF) * K1_CHUNK + jd[jj]);
  };
  u32x2 bias_v = {0u, 0u};
  if (NH > 0 && tid < NH * 4) bias_v = *(const u32x2*)(b1 + (c0 + 16 * NAB - 1024) + 4 * tid);
  issue(0);
  issue(1);
  issue(2);

  f32x4 acc[2][NBW], cs[NBW];
#pragma unroll
  for (int b = 0; b < NBW; ++b) {
    acc[0][b] = (f32x4){0.f, 0.f, 0.f, 0.f};
    acc[1][b] = (f32x4){0.f, 0.f, 0.f, 0.f};
    cs[b] = (f32x4){0.f, 0.f, 0.f, 0.f};
  }
  f32x4 d1 = {0.f, 0.f, 0.f, 0.f}, d2 = {0.f, 0.f, 0.f, 0.f};
  bf16x8 ones = {0x3f80, 0x3f80, 0x3f80, 0x3f80, 0x3f80, 0x3f80, 0x3f80, 0x3f80};
  asm volatile("" : "+v"(ones));

  const int q4 = li >> 2, p4 = lane & 3;
  const int laneX0 = (wm * 32 + 8 * q4 + (li & 3)) * 128 + ((0 + g) ^ (li >> 1)) * 16;
  const int laneX1 = (wm * 32 + 8 * q4 + (li & 3)) * 128 + ((4 + g) ^ (li >> 1)) * 16;
  const int laneAB0 = 16384 + li * 128 + ((0 + g) ^ (li >> 1)) * 16;
  const int laneAB1 = 16384 + li * 128 + ((4 + g) ^ (li >> 1)) * 16;
  const int laneH0 = 16384 + NAB * 2048 + (8 * g + q4) * (NHD * 32) + (g & 1) * ROT * 32 + p4 * 8;
  const int laneH1 = laneH0 - (g & 1) * NH * 32;
  int boff = 0;
  unsigned pf0 = 0, pf1 = 0;
  auto setbuf = [&](int kc) {
    boff = (kc % K1_NBUF) * K1_CHUNK;
    asm volatile("" : "+s"(boff));
  };
  bf16x8 wf[NBW], xa[2][2];
  auto rdx = [&](int step) {
    const int ks = step & 1;
    xa[ks][0] = *(const bf16x8*)(smem + boff + (ks ? laneX1 : laneX0));
    xa[ks][1] = *(const bf16x8*)(smem + boff + (ks ? laneX1 : laneX0) + 512);
  };
  auto rdw = [&](int step, int bb) {
    const int ks = step & 1;
    const int b = wn * NB0 + bb;
    if (b < NAB) {
      wf[bb] = *(const bf16x8*)(smem + boff + (ks ? laneAB1 : laneAB0) + b * 2048);
    } else {
      const int hb = b - NAB;
      const char* hp = smem + boff + ((hb + ROT >= NH) ? laneH1 : laneH0) + 32 * ks * (NHD * 32) + hb * 32;
      const bf16x4 t0 = __builtin_amdgcn_ds_read_tr16_b64_v4i16((__attribute__((address_space(3))) bf16x4*)(hp));
      const bf16x4 t1 = __builtin_amdgcn_ds_read_tr16_b64_v4i16((__attribute__((address_space(3))) bf16x4*)(hp + 4 * (NHD * 32)));
      wf[bb] = __builtin_shufflevector(t0, t1, 0, 1, 2, 3, 4, 5, 6, 7);
    }
  };
#pragma unroll
  for (int bb = 0; bb < NBW; ++bb) asm volatile("s_nop 1" : "+v"(acc[0][bb]), "+v"(acc[1][bb]), "+v"(cs[bb]));
  asm volatile("s_nop 1" : "+v"(d1), "+v"(d2));
  wait_vm<10>();
  BAR();
  STAMP(1);
  setbuf(0);
  rdx(0);
#pragma unroll
  for (int bb = 0; bb < NBW; ++bb) rdw(0, bb);
#pragma unroll
  for (int step = 0; step < 16; ++step) {
    const int kc = step >> 1, ks = step & 1;
    if (ks && kc < 7) {
      if (PF_KC == 4) { if (kc <= 4) wait_vm<5>(); else if (kc == 5) wait_vm<7>(); else wait_vm<2>(); }
      else { if (kc <= 5) wait_vm<5>(); else wait_vm<0>(); }
      BAR();
      if (kc + 3 < 8) issue(kc + 3);
      if (kc == PF_KC) {
        pf0 = *(const unsigned*)(pf);
        pf1 = *(const unsigned*)(pf + 32768);
      }
      setbuf(kc + 1);
      STAMP(4 + kc);
    }
    if (step < 15) rdx(step + 1);
    asm("v_mfma_f32_16x16x32_bf16 %0, %2, %3, %0\n\tv_mfma_f32_16x16x32_bf16 %1, %2, %2, %1"
        : "+v"(d1), "+v"(d2)
        : "v"(xa[ks][wn]), "v"(ones));
    __builtin_amdgcn_sched_barrier(0);
#pragma unroll
    for (int bb = 0; bb < NBW; ++bb) {
      const int b = wn * NB0 + bb;
      if (b < NAB) {
        asm("v_mfma_f32_16x16x32_bf16 %0, %3, %5, %0\n\tv_mfma_f32_16x16x32_bf16 %1, %4, %5, %1\n\t"
            "v_mfma_f32_16x16x32_bf16 %2, %6, %5, %2"
            : "+v"(acc[0][bb]), "+v"(acc[1][bb]), "+v"(cs[bb])
            : "v"(xa[ks][0]), "v"(xa[ks][1]), "v"(wf[bb]), "v"(ones));
      } else {
        asm("v_mfma_f32_16x16x32_bf16 %0, %5, %3, %0\n\tv_mfma_f32_16x16x32_bf16 %1, %5, %4, %1\n\t"
            "v_mfma_f32_16x16x32_bf16 %2, %5, %6, %2"
            : "+v"(acc[0][bb]), "+v"(acc[1][bb]), "+v"(cs[bb])
            : "v"(xa[ks][0]), "v"(xa[ks][1]), "v"(wf[bb]), "v"(ones));
      }
      if (step < 15) rdw(step + 1, bb);
      __builtin_amdgcn_sched_barrier(0);
    }
  }
  STAMP(12);

  {
    asm volatile("s_nop 7" : "+v"(d1), "+v"(d2));
    const bool valid = q4 == g;
    const int rr = li & 3;
    const float s = rr == 0 ? d1[0] : rr == 1 ? d1[1] : rr == 2 ? d1[2] : d1[3];
    const float q = rr == 0 ? d2[0] : rr == 1 ? d2[1] : rr == 2 ? d2[2] : d2[3];
    float mean = amul(s, 1.0f / 512.0f);
    float varf = afma(q, 1.0f / 512.0f, -mean * mean);
    const int srow = 8 * q4 + 4 * wn + rr;
    if (__any(valid && amul(mean, mean) > amul(8.0f, varf))) {
      mean = __shfl(mean, q4 * 16 + li);
      float v0 = 0.f, v1 = 0.f;
      for (int ks = 0; ks < 16; ++ks) {
        const u32x4 v = *(const u32x4*)(x + (m0 + srow) * DM + ks * 32 + g * 8);
#pragma unroll
        for (int qq = 0; qq < 4; ++qq) {
          const float e0 = aadd(bf_lo(v[qq]), -mean), e1 = aadd(bf_hi(v[qq]), -mean);
          v0 = afma(e0, e0, v0);
          v1 = afma(e1, e1, v1);
        }
      }
      float vs = v0 + v1;
      vs += __shfl_xor(vs, 16);
      vs += __shfl_xor(vs, 32);
      varf = vs * (1.0f / 512.0f);
    }
    const float var = rbf(varf);
    const float sd = rbf(sqrtf(rbf(var + 1e-5f)));
    const float rinv = 1.0f / sd;
    const float mb16 = rbf(mean);
    if (valid) ((float2*)(smem + K1_STATS))[wm * 32 + srow] = make_float2(rinv, -mb16 * rinv);
    if (NH > 0 && tid < NH * 4) *(u32x2*)(smem + K1_BIAS + tid * 8) = bias_v;
  }
  WAIT_L0();
  BAR();

  const __amdgpu_buffer_rsrc_t rsB = __builtin_amdgcn_make_buffer_rsrc((void*)BT, 0, DM * KTOT * 2, 0x00020000);
  const float2* stp = (const float2*)(smem + K1_STATS) + wm * 32;
  float2 sta[8];
  float2 sth[2];
  if (NAB > wn * NB0) {
#pragma unroll
    for (int i = 0; i < 8; ++i) sta[i] = stp[8 * g + i];
  }
  if (NAB < 11 && !(wn == 0 && NAB >= NB0)) {
#pragma unroll
    for (int mb = 0; mb < 2; ++mb) sth[mb] = stp[8 * q4 + 4 * mb + (li & 3)];
  }
#pragma unroll
  for (int bb = 0; bb < NBW; ++bb) {
    const int b = wn * NB0 + bb;
    if (b < NAB) {
      const int n = c0 + 16 * b + li;
      const bool isb = n >= 512;
      const unsigned sgn = isb ? 0x80008000u : 0u;
      u32x4 o;
#pragma unroll
      for (int mb = 0; mb < 2; ++mb) {
        float t0, t1, t2, t3;
        unsigned o0, o1;
        asm("v_mul_f32 %2, %11, %10\n\tv_mul_f32 %3, %13, %10\n\tv_mul_f32 %4, %15, %10\n\tv_mul_f32 %5, %17, %10\n\t"
            "v_fma_f32 %2, %12, %6, %2\n\tv_fma_f32 %3, %14, %7, %3\n\tv_fma_f32 %4, %16, %8, %4\n\tv_fma_f32 %5, %18, %9, %5\n\t"
            "v_cvt_pk_bf16_f32 %0, %2, %3\n\tv_cvt_pk_bf16_f32 %1, %4, %5\n\t"
            "v_xor_b32 %0, %19, %0\n\tv_xor_b32 %1, %19, %1"
            : "=&v"(o0), "=&v"(o1), "=&v"(t0), "=&v"(t1), "=&v"(t2), "=&v"(t3)
            : "v"(acc[mb][bb][0]), "v"(acc[mb][bb][1]), "v"(acc[mb][bb][2]), "v"(acc[mb][bb][3]), "v"(cs[bb][0]),
              "v"(sta[4 * mb + 0].y), "v"(sta[4 * mb + 0].x), "v"(sta[4 * mb + 1].y), "v"(sta[4 * mb + 1].x),
              "v"(sta[4 * mb + 2].y), "v"(sta[4 * mb + 2].x), "v"(sta[4 * mb + 3].y), "v"(sta[4 * mb + 3].x),
              "v"(sgn));
        o[2 * mb] = o0;
        o[2 * mb + 1] = o1;
      }
      __builtin_amdgcn_raw_buffer_store_b128(o, rsB, ((n & 511) * KTOT + (isb ? 2048 : 0) + m0 + 8 * g) * 2, 0, 0);
    } else {
      const int fl = 16 * (b - NAB) + 4 * g;
      const u32x2 bv = *(const u32x2*)(smem + K1_BIAS + fl * 2);
      const float bias0 = bf_lo(bv[0]), bias1 = bf_hi(bv[0]), bias2 = bf_lo(bv[1]), bias3 = bf_hi(bv[1]);
#pragma unroll
      for (int mb = 0; mb < 2; ++mb) {
        float t0, t1, t2, t3;
        unsigned o0, o1;
        asm("v_fma_f32 %2, %19, %10, %14\n\tv_fma_f32 %3, %19, %11, %15\n\tv_fma_f32 %4, %19, %12, %16\n\tv_fma_f32 %5, %19, %13, %17\n\t"
            "v_fma_f32 %2, %18, %6, %2\n\tv_fma_f32 %3, %18, %7, %3\n\tv_fma_f32 %4, %18, %8, %4\n\tv_fma_f32 %5, %18, %9, %5\n\t"
            "v_max_f32 %2, 0, %2\n\tv_max_f32 %3, 0, %3\n\tv_max_f32 %4, 0, %4\n\tv_max_f32 %5, 0, %5\n\t"
            "v_cvt_pk_bf16_f32 %0, %2, %3\n\tv_cvt_pk_bf16_f32 %1, %4, %5\n\t"
            "v_lshlrev_b32 %2, 16, %0\n\tv_and_b32 %3, 0xffff0000, %0\n\tv_lshlrev_b32 %4, 16, %1\n\tv_and_b32 %5, 0xffff0000, %1\n\t"
            "v_mul_f32 %2, %2, %2\n\tv_mul_f32 %3, %3, %3\n\tv_mul_f32 %4, %4, %4\n\tv_mul_f32 %5, %5, %5\n\t"
            "v_cvt_pk_bf16_f32 %0, %2, %3\n\tv_cvt_pk_bf16_f32 %1, %4, %5"
            : "=&v"(o0), "=&v"(o1), "=&v"(t0), "=&v"(t1), "=&v"(t2), "=&v"(t3)
            : "v"(acc[mb][bb][0]), "v"(acc[mb][bb][1]), "v"(acc[mb][bb][2]), "v"(acc[mb][bb][3]),
              "v"(cs[bb][0]), "v"(cs[bb][1]), "v"(cs[bb][2]), "v"(cs[bb][3]),
              "v"(bias0), "v"(bias1), "v"(bias2), "v"(bias3), "v"(sth[mb].x), "v"(sth[mb].y));
        u32x2 o;
        o[0] = o0;
        o[1] = o1;
        const int row = wm * 32 + 8 * q4 + 4 * mb + (li & 3);
        *(u32x2*)(smem + row * (NHD * 32) + fl * 2) = o;
      }
    }
  }
  if (NH > 0) {
    WAIT_L0();
    BAR();
    const __amdgpu_buffer_rsrc_t rsH = __builtin_amdgcn_make_buffer_rsrc((void*)H, 0, SEQ * FF * 2, 0x00020000);
    const int hoff = ((mt * 128) * FF + (c0 + 16 * NAB - 1024)) * 2;
#pragma unroll
    for (int i = 0; i < (128 * NHD * 2 + 511) / 512; ++i) {
      const unsigned id = tid + 512 * i;
      if (128 * NHD * 2 % 512 == 0 || id < 128 * NHD * 2) {
        const unsigned row = id / (NHD * 2), c = id - row * (NHD * 2);
        __builtin_amdgcn_raw_buffer_store_b128(*(const u32x4*)(smem + id * 16), rsH, hoff + (row * FF + c * 8) * 2, 0, 0);
      }
    }
  }
  asm volatile("" ::"v"(pf0), "v"(pf1));
}

__global__ __launch_bounds__(512, 2) void k1(const u16* __restrict__ x, const u16* __restrict__ Wr,
                                             const u16* __restrict__ Wi, const u16* __restrict__ w1,
                                             const u16* __restrict__ b1, const u16* __restrict__ w2,
                                             u16* __restrict__ BT, u16* __restrict__ H,
                                             unsigned* __restrict__ cnt, const u16* __restrict__ Cr,
                                             const u16* __restrict__ Ci) {
  extern __shared__ __attribute__((aligned(16))) char smem[];
  const int bid = blockIdx.x;
  const int xcd = bid & 7, idx = bid >> 3;
  const int mt = (xcd & 1) * 8 + (idx >> 2);
  const int nt = (xcd >> 1) * 4 + (idx & 3);
  const int tid = threadIdx.x;

  RSTAMP(0, 30);
  const int tf0 = (bid >> 3) * 64, te0 = (bid & 7) * 64;
  u32x4 w2v = {0u, 0u, 0u, 0u};
  if (bid < 224) w2v = *(const u32x4*)(w2 + (tf0 + (tid >> 3)) * DM + te0 + (tid & 7) * 8);
  if (bid == 0 && tid < 64) cnt[tid * 64] = 0u;

  const char* pf = (const char*)((bid < 128) ? Cr : Ci) + (size_t)(bid & 127) * 65536 + tid * 64;
  const int wn = __builtin_amdgcn_readfirstlane(tid >> 8);
  if (wn == 0) {
    if (nt <= 4) k1_body<11, 0>(x, Wr, Wi, w1, b1, BT, H, smem, mt, nt, pf);
    else if (nt == 5) k1_body<9, 0>(x, Wr, Wi, w1, b1, BT, H, smem, mt, nt, pf);
    else k1_body<0, 0>(x, Wr, Wi, w1, b1, BT, H, smem, mt, nt, pf);
  } else {
    if (nt <= 4) k1_body<11, 1>(x, Wr, Wi, w1, b1, BT, H, smem, mt, nt, pf);
    else if (nt == 5) k1_body<9, 1>(x, Wr, Wi, w1, b1, BT, H, smem, mt, nt, pf);
    else k1_body<0, 1>(x, Wr, Wi, w1, b1, BT, H, smem, mt, nt, pf);
  }
  STAMP(13);
  if (bid < 224) {
    u16* T = (u16*)(smem + 65536);
    const int f = tid >> 3, e8 = (tid & 7) * 8;
#pragma unroll
    for (int q = 0; q < 4; ++q) {
      T[(e8 + 2 * q) * 72 + f] = (u16)(w2v[q] & 0xffffu);
      T[(e8 + 2 * q + 1) * 72 + f] = (u16)(w2v[q] >> 16);
    }
    __syncthreads();
    const int e = tid >> 3, f8 = (tid & 7) * 8;
    const u32x4 v = *(const u32x4*)(T + e * 72 + f8);
    *(u32x4*)(BT + (te0 + e) * KTOT + 4096 + tf0 + f8) = v;
  }
  RSTAMP(0, 31);
}

#define K2_NBUF 4
#define K2_TILE 32768
#define K2_LDS (K2_NBUF * K2_TILE)
#define K2_NT 23

template <int SK>
__device__ __forceinline__ void k2_epi(f32x4 (&r)[2][4], float* __restrict__ slabs, unsigned* __restrict__ cnt,
                                       const u16* __restrict__ x, const u16* __restrict__ b2,
                                       u16* __restrict__ out, int tile, int row0, int col0, int w) {
  const int tid = threadIdx.x, lane = tid & 63;
  const int li = lane & 15, g = lane >> 4;
  const __amdgpu_buffer_rsrc_t rs = __builtin_amdgcn_make_buffer_rsrc((void*)slabs, 0, 64 * 16 * 8192, 0x00020000);
#pragma unroll
  for (int d = 0; d < 4; ++d) {
    if (d == SK) continue;
    const f32x4 p0 = r[d >> 1][2 * (d & 1)], p1 = r[d >> 1][2 * (d & 1) + 1];
    u32x4 o;
    o[0] = pack2(p0[0], p0[1]);
    o[1] = pack2(p0[2], p0[3]);
    o[2] = pack2(p1[0], p1[1]);
    o[3] = pack2(p1[2], p1[3]);
    __builtin_amdgcn_raw_buffer_store_b128(o, rs, ((tile * 4 + d) * 4 + SK) * 8192 + w * 1024 + lane * 16, 0, 16);
  }
  const int row = row0 + (SK >> 1) * 16 + li;
  const int colb = col0 + (SK & 1) * 32 + 4 * g;
  u32x2 xv[2], bv[2];
#pragma unroll
  for (int j = 0; j < 2; ++j) {
    xv[j] = *(const u32x2*)(x + row * DM + colb + j * 16);
    bv[j] = *(const u32x2*)(b2 + colb + j * 16);
  }
  WAIT_V(0);
  STAMP2(6);
  __syncthreads();
  if (tid == 0) {
    unsigned* c = cnt + tile * 64;
    __hip_atomic_fetch_add(c, 1u, __ATOMIC_RELAXED, __HIP_MEMORY_SCOPE_AGENT);
    unsigned spins = 0;
    while (__hip_atomic_load(c, __ATOMIC_RELAXED, __HIP_MEMORY_SCOPE_AGENT) < 4u) {
      __builtin_amdgcn_s_sleep(1);
      if (++spins > (1u << 24)) break;
    }
  }
  __syncthreads();
  STAMP2(7);
  u32x4 part[4];
#pragma unroll
  for (int s = 0; s < 4; ++s)
    if (s != SK) part[s] = __builtin_amdgcn_raw_buffer_load_b128(rs, ((tile * 4 + SK) * 4 + s) * 8192 + w * 1024 + lane * 16, 0, 16);
#pragma unroll
  for (int j = 0; j < 2; ++j) {
    float sum[4];
#pragma unroll
    for (int i = 0; i < 4; ++i) {
      float t = r[SK >> 1][2 * (SK & 1) + j][i];
#pragma unroll
      for (int s = 0; s < 4; ++s)
        if (s != SK) t = aadd(t, (i & 1) ? bf_hi(part[s][2 * j + (i >> 1)]) : bf_lo(part[s][2 * j + (i >> 1)]));
      sum[i] = t;
    }
    u32x2 o;
    o[0] = pack2(aadd(aadd(bf_lo(xv[j][0]), sum[0]), bf_lo(bv[j][0])), aadd(aadd(bf_hi(xv[j][0]), sum[1]), bf_hi(bv[j][0])));
    o[1] = pack2(aadd(aadd(bf_lo(xv[j][1]), sum[2]), bf_lo(bv[j][1])), aadd(aadd(bf_hi(xv[j][1]), sum[3]), bf_hi(bv[j][1])));
    *(u32x2*)(out + row * DM + colb + j * 16) = o;
  }
  STAMP2(8);
}

__global__ __launch_bounds__(512, 2) void k2(const u16* __restrict__ Cr, const u16* __restrict__ Ci,
                                             const u16* __restrict__ H, const u16* __restrict__ BT,
                                             const u16* __restrict__ x, const u16* __restrict__ b2,
                                             u16* __restrict__ out, float* __restrict__ slabs,
                                             unsigned* __restrict__ cnt) {
  extern __shared__ __attribute__((aligned(16))) char smem[];
  const int bid = blockIdx.x;
  const int xcd = bid & 7, idx = bid >> 3;
  const int sk = xcd >> 1;
  const int mt = (xcd & 1) * 8 + (idx >> 2);
  const int nt = idx & 3;
  const int tid = threadIdx.x, lane = tid & 63;
  const int w = __builtin_amdgcn_readfirstlane(tid >> 6);
  const int ksel = w >> 2, wm = (w >> 1) & 1, wn = w & 1;
  const int li = lane & 15, g = lane >> 4;
  const int m0 = mt * 128, n0 = nt * 128;

  RSTAMP(1, 30);
  const int row0 = 16 * w + (lane >> 3);
  const int chE = ((lane & 7) ^ (lane >> 4)) * 8, chO = chE ^ 32;
  const int kt0 = sk * K2_NT;
  const u16* bsrc = BT + (n0 + row0) * KTOT;
  auto issue = [&](int t) {
    const int kt = kt0 + t;
    char* dst = smem + (t % K2_NBUF) * K2_TILE + w * 2048;
    const u16* ap = (kt < 32) ? (Cr + kt * 64) : (kt < 64) ? (Ci + (kt - 32) * 64) : (H + (kt - 64) * 64);
    const int pitch = (kt < 64) ? 2048 : FF;
    const u16* asrc = ap + (m0 + row0) * pitch;
    glds16(asrc + chE, dst);
    glds16(asrc + 8 * pitch + chO, dst + 1024);
    const u16* bp = bsrc + kt * 64;
    glds16(bp + chE, dst + 16384);
    glds16(bp + 8 * KTOT + chO, dst + 16384 + 1024);
  };

  f32x4 acc[4][4];
#pragma unroll
  for (int i = 0; i < 4; ++i)
#pragma unroll
    for (int j = 0; j < 4; ++j) acc[i][j] = (f32x4){0.f, 0.f, 0.f, 0.f};

  const int rdA = (wm * 64 + li) * 128 + ((4 * ksel + g) ^ (li >> 1)) * 16;
  const int rdB = 16384 + (wn * 64 + li) * 128 + ((4 * ksel + g) ^ (li >> 1)) * 16;

  bf16x8 af[4], bfr[4];
  auto rd = [&](int t) {
    const char* buf = smem + (t % K2_NBUF) * K2_TILE;
#pragma unroll
    for (int i = 0; i < 4; ++i) bfr[i] = *(const bf16x8*)(buf + rdB + i * 2048);
#pragma unroll
    for (int i = 0; i < 4; ++i) af[i] = *(const bf16x8*)(buf + rdA + i * 2048);
  };
  auto mm = [&]() {
#pragma unroll
    for (int i = 0; i < 4; ++i)
#pragma unroll
      for (int j = 0; j < 4; ++j)
        acc[i][j] = __builtin_amdgcn_mfma_f32_16x16x32_bf16(bfr[j], af[i], acc[i][j], 0, 0, 0);
  };
  STAMP2(0);
  issue(0);
  issue(1);
  issue(2);
  if (w < 4) {
    for (int t = 0; t < K2_NT; ++t) {
      if (t + 2 < K2_NT) wait_vm<8>(); else if (t + 1 < K2_NT) wait_vm<4>(); else wait_vm<0>();
      WAIT_L0();
      BAR();
      if (t == 0) STAMP2(1); if (t == 4) STAMP2(2); if (t == 12) STAMP2(3); if (t == 20) STAMP2(4);
      if (t + 3 < K2_NT) issue(t + 3);
      rd(t);
      __builtin_amdgcn_sched_barrier(0);
      mm();
      __builtin_amdgcn_sched_barrier(0);
    }
  } else {
    wait_vm<8>();
    BAR();
    STAMP2(1);
    issue(3);
    rd(0);
    for (int t = 1; t < K2_NT; ++t) {
      if (t + 2 < K2_NT) wait_vm<8>(); else if (t + 1 < K2_NT) wait_vm<4>(); else wait_vm<0>();
      WAIT_L0();
      BAR();
      if (t == 4) STAMP2(2); if (t == 12) STAMP2(3); if (t == 20) STAMP2(4);
      mm();
      __builtin_amdgcn_sched_barrier(0);
      if (t + 3 < K2_NT) issue(t + 3);
      rd(t);
      __builtin_amdgcn_sched_barrier(0);
    }
    mm();
  }
  STAMP2(5);
  f32x4 r[2][4];
  WAIT_L0();
  BAR();
  {
    char* mine = smem + w * 8192 + lane * 16;
    const char* theirs = smem + (w ^ 4) * 8192 + lane * 16;
    if (ksel == 0) {
#pragma unroll
      for (int i = 0; i < 2; ++i)
#pragma unroll
        for (int j = 0; j < 4; ++j) { *(f32x4*)(mine + (i * 4 + j) * 1024) = acc[2 + i][j]; r[i][j] = acc[i][j]; }
    } else {
#pragma unroll
      for (int i = 0; i < 2; ++i)
#pragma unroll
        for (int j = 0; j < 4; ++j) { *(f32x4*)(mine + (i * 4 + j) * 1024) = acc[i][j]; r[i][j] = acc[2 + i][j]; }
    }
    WAIT_L0();
    BAR();
    f32x4 o[2][4];
#pragma unroll
    for (int i = 0; i < 2; ++i)
#pragma unroll
      for (int j = 0; j < 4; ++j) o[i][j] = *(const f32x4*)(theirs + (i * 4 + j) * 1024);
#pragma unroll
    for (int i = 0; i < 2; ++i)
#pragma unroll
      for (int j = 0; j < 4; ++j)
#pragma unroll
        for (int q = 0; q < 4; ++q) r[i][j][q] = aadd(r[i][j][q], o[i][j][q]);
  }
  const int tile = mt * 4 + nt;
  const int rw0 = m0 + wm * 64 + ksel * 32, cl0 = n0 + wn * 64;
  if (sk == 0) k2_epi<0>(r, slabs, cnt, x, b2, out, tile, rw0, cl0, w);
  else if (sk == 1) k2_epi<1>(r, slabs, cnt, x, b2, out, tile, rw0, cl0, w);
  else if (sk == 2) k2_epi<2>(r, slabs, cnt, x, b2, out, tile, rw0, cl0, w);
  else k2_epi<3>(r, slabs, cnt, x, b2, out, tile, rw0, cl0, w);
  RSTAMP(1, 31);
}

extern "C" void kernel_launch(void* const* d_in, const int* in_sizes, int n_in, void* d_out, int out_size,
                              void* d_ws, size_t ws_size, hipStream_t stream) {
  const u16* x = (const u16*)d_in[0];
  const u16* Cr = (const u16*)d_in[1];
  const u16* Ci = (const u16*)d_in[2];
  const u16* Wr = (const u16*)d_in[3];
  const u16* Wi = (const u16*)d_in[4];
  const u16* w1 = (const u16*)d_in[6];
  const u16* b1 = (const u16*)d_in[7];
  const u16* w2 = (const u16*)d_in[8];
  const u16* b2 = (const u16*)d_in[9];
  u16* out = (u16*)d_out;
  char* ws = (char*)d_ws;
  u16* BT = (u16*)ws;
  u16* H = (u16*)(ws + 6029312);
  float* slabs = (float*)(ws + 6029312 + 7340032);
  unsigned* cnt = (unsigned*)(ws + 6029312 + 7340032 + 16777216);

  (void)hipFuncSetAttribute((const void*)k1, hipFuncAttributeMaxDynamicSharedMemorySize, K1_LDS);
  (void)hipFuncSetAttribute((const void*)k2, hipFuncAttributeMaxDynamicSharedMemorySize, K2_LDS);
  k1<<<256, 512, K1_LDS, stream>>>(x, Wr, Wi, w1, b1, w2, BT, H, cnt, Cr, Ci);
  k2<<<256, 512, K2_LDS, stream>>>(Cr, Ci, H, BT, x, b2, out, slabs, cnt);
}
```

```cpp
#include <hip/hip_runtime.h>
#include <hip/hip_bf16.h>
#include <stdint.h>

typedef unsigned short u16;
typedef __attribute__((ext_vector_type(8))) short bf16x8;
typedef __attribute__((ext_vector_type(4))) short bf16x4;
typedef __attribute__((ext_vector_type(2))) short bf16x2;
typedef __attribute__((ext_vector_type(4))) float f32x4;
typedef __attribute__((ext_vector_type(4))) unsigned int u32x4;
typedef __attribute__((ext_vector_type(2))) unsigned int u32x2;

#define SEQ 2048
#define DM 512
#define FF 1792
#define KTOT 5888

#define STAMP(i)
#define STAMP2(i)
#define RSTAMP(k, i)
#define WAIT_V(n) asm volatile("s_waitcnt vmcnt(" #n ")" ::: "memory")
#define WAIT_L0() asm volatile("s_waitcnt lgkmcnt(0)" ::: "memory")
#define BAR() asm volatile("s_barrier" ::: "memory")

__device__ __forceinline__ float bf2f(u16 v) { return __uint_as_float(((unsigned)v) << 16); }
__device__ __forceinline__ float bf_lo(unsigned v) { return __uint_as_float(v << 16); }
__device__ __forceinline__ float bf_hi(unsigned v) { return __uint_as_float(v & 0xffff0000u); }
__device__ __forceinline__ unsigned pack2(float lo, float hi) {
  unsigned r;
  asm("v_cvt_pk_bf16_f32 %0, %1, %2" : "=v"(r) : "v"(lo), "v"(hi));
  return r;
}
__device__ __forceinline__ float rbf(float f) { return bf_lo(pack2(f, f)); }
__device__ __forceinline__ float afma(float a, float b, float c) {
  float r;
  asm("v_fma_f32 %0, %1, %2, %3" : "=v"(r) : "v"(a), "v"(b), "v"(c));
  return r;
}
__device__ __forceinline__ float aadd(float a, float b) {
  float r;
  asm("v_add_f32 %0, %1, %2" : "=v"(r) : "v"(a), "v"(b));
  return r;
}
__device__ __forceinline__ float amul(float a, float b) {
  float r;
  asm("v_mul_f32 %0, %1, %2" : "=v"(r) : "v"(a), "v"(b));
  return r;
}

template <int N> __device__ __forceinline__ void wait_vm() {
  asm volatile("s_waitcnt vmcnt(%0)" ::"i"(N) : "memory");
}

__device__ __forceinline__ void glds16(const void* g, void* l) {
  __builtin_amdgcn_global_load_lds((const __attribute__((address_space(1))) unsigned*)g,
                                   (__attribute__((address_space(3))) unsigned*)l, 16, 0, 0);
}

#define PF_KC 4
#define K1_CHUNK 38912
#define K1_NBUF 4
#define K1_STATS (K1_NBUF * K1_CHUNK)
#define K1_BIAS (K1_STATS + 1024)
#define K1_LDS (K1_BIAS + 512)

template <int NAB, int WN>
__device__ __forceinline__ void k1_body(const u16* __restrict__ x, const u16* __restrict__ Wr,
                                        const u16* __restrict__ Wi, const u16* __restrict__ w1,
                                        const u16* __restrict__ b1, u16* __restrict__ BT,
                                        u16* __restrict__ H, char* smem, int mt, int nt,
                                        const char* pf) {
  constexpr int NH = 11 - NAB;
  constexpr int ROT = (NH == 11) ? 4 : 1;
  constexpr int NHD = (NH == 0) ? 1 : NH;
  constexpr int NB0 = 6;
  constexpr int wn = WN;
  constexpr int NBW = (wn == 0) ? 6 : 5;
  const int tid = threadIdx.x;
  const int lane = tid & 63;
  const int w = __builtin_amdgcn_readfirstlane(tid >> 6);
  const int wm = w & 3;
  const int li = lane & 15, g = lane >> 4;
  const int c0 = nt * 176;
  const int m0 = mt * 128 + wm * 32;

  STAMP(0);
  const u16* src[5];
  int inc[5], jd[5];
#pragma unroll
  for (int jj = 0; jj < 2; ++jj) {
    const int row = 16 * w + 8 * jj + (lane >> 3);
    const int sw = (((row >> 3) & 3) << 1) | ((row >> 1) & 1);
    src[jj] = x + (mt * 128 + row) * DM + ((lane & 7) ^ sw) * 8;
    inc[jj] = 64;
    jd[jj] = (2 * w + jj) * 1024;
  }
#pragma unroll
  for (int jj = 0; jj < 3; ++jj) {
    int j = w + 8 * jj;
    if (j > 21) j -= 8;
    jd[2 + jj] = 16384 + j * 1024;
    if (NAB == 11 || (NAB > 0 && j < 2 * NAB)) {
      const int row = 8 * j + (lane >> 3);
      const int ch = (lane & 7) ^ ((row >> 1) & 7);
      const int n = c0 + row;
      const u16* base = (n < 512) ? (Wr + n * DM) : (Wi + (n - 512) * DM);
      src[2 + jj] = base + ch * 8;
      inc[2 + jj] = 64;
    } else {
      const unsigned p16 = (j - 2 * NAB) * 64 + lane;
      const unsigned r = p16 / (2 * NHD);
      const unsigned rem = p16 - r * (2 * NHD);
      const int cp = rem >> 1, hf = rem & 1;
      int c = cp - ROT * ((r >> 3) & 1);
      if (c < 0) c += NH;
      src[2 + jj] = w1 + r * FF + (c0 + 16 * NAB - 1024) + 16 * c + 8 * hf;
      inc[2 + jj] = 64 * FF;
    }
  }
  auto issue = [&](int kc) {
#pragma unroll
    for (int jj = 0; jj < 5; ++jj) glds16(src[jj] + kc * inc[jj], smem + (kc % K1_NBUF) * K1_CHUNK + jd[jj]);
  };
  u32x2 bias_v = {0u, 0u};
  if (NH > 0 && tid < NH * 4) bias_v = *(const u32x2*)(b1 + (c0 + 16 * NAB - 1024) + 4 * tid);
  issue(0);
  issue(1);
  issue(2);

  f32x4 acc[2][NBW], cs[NBW];
#pragma unroll
  for (int b = 0; b < NBW; ++b) {
    acc[0][b] = (f32x4){0.f, 0.f, 0.f, 0.f};
    acc[1][b] = (f32x4){0.f, 0.f, 0.f, 0.f};
    cs[b] = (f32x4){0.f, 0.f, 0.f, 0.f};
  }
  f32x4 d1 = {0.f, 0.f, 0.f, 0.f}, d2 = {0.f, 0.f, 0.f, 0.f};
  bf16x8 ones = {0x3f80, 0x3f80, 0x3f80, 0x3f80, 0x3f80, 0x3f80, 0x3f80, 0x3f80};
  asm volatile("" : "+v"(ones));

  const int q4 = li >> 2, p4 = lane & 3;
  const int laneX0 = (wm * 32 + 8 * q4 + (li & 3)) * 128 + ((0 + g) ^ (li >> 1)) * 16;
  const int laneX1 = (wm * 32 + 8 * q4 + (li & 3)) * 128 + ((4 + g) ^ (li >> 1)) * 16;
  const int laneAB0 = 16384 + li * 128 + ((0 + g) ^ (li >> 1)) * 16;
  const int laneAB1 = 16384 + li * 128 + ((4 + g) ^ (li >> 1)) * 16;
  const int laneH0 = 16384 + NAB * 2048 + (8 * g + q4) * (NHD * 32) + (g & 1) * ROT * 32 + p4 * 8;
  const int laneH1 = laneH0 - (g & 1) * NH * 32;
  int boff = 0;
  unsigned pf0 = 0, pf1 = 0;
  auto setbuf = [&](int kc) {
    boff = (kc % K1_NBUF) * K1_CHUNK;
    asm volatile("" : "+s"(boff));
  };
  bf16x8 wf[NBW], xa[2][2];
  auto rdx = [&](int step) {
    const int ks = step & 1;
    xa[ks][0] = *(const bf16x8*)(smem + boff + (ks ? laneX1 : laneX0));
    xa[ks][1] = *(const bf16x8*)(smem + boff + (ks ? laneX1 : laneX0) + 512);
  };
  auto rdw = [&](int step, int bb) {
    const int ks = step & 1;
    const int b = wn * NB0 + bb;
    if (b < NAB) {
      wf[bb] = *(const bf16x8*)(smem + boff + (ks ? laneAB1 : laneAB0) + b * 2048);
    } else {
      const int hb = b - NAB;
      const char* hp = smem + boff + ((hb + ROT >= NH) ? laneH1 : laneH0) + 32 * ks * (NHD * 32) + hb * 32;
      const bf16x4 t0 = __builtin_amdgcn_ds_read_tr16_b64_v4i16((__attribute__((address_space(3))) bf16x4*)(hp));
      const bf16x4 t1 = __builtin_amdgcn_ds_read_tr16_b64_v4i16((__attribute__((address_space(3))) bf16x4*)(hp + 4 * (NHD * 32)));
      wf[bb] = __builtin_shufflevector(t0, t1, 0, 1, 2, 3, 4, 5, 6, 7);
    }
  };
#pragma unroll
  for (int bb = 0; bb < NBW; ++bb) asm volatile("s_nop 1" : "+v"(acc[0][bb]), "+v"(acc[1][bb]), "+v"(cs[bb]));
  asm volatile("s_nop 1" : "+v"(d1), "+v"(d2));
  wait_vm<10>();
  BAR();
  STAMP(1);
  setbuf(0);
  rdx(0);
#pragma unroll
  for (int bb = 0; bb < NBW; ++bb) rdw(0, bb);
#pragma unroll
  for (int step = 0; step < 16; ++step) {
    const int kc = step >> 1, ks = step & 1;
    if (ks && kc < 7) {
      if (PF_KC == 4) { if (kc <= 4) wait_vm<5>(); else if (kc == 5) wait_vm<7>(); else wait_vm<2>(); }
      else { if (kc <= 5) wait_vm<5>(); else wait_vm<0>(); }
      BAR();
      if (kc + 3 < 8) issue(kc + 3);
      if (kc == PF_KC) {
        pf0 = *(const unsigned*)(pf);
        pf1 = *(const unsigned*)(pf + 32768);
      }
      setbuf(kc + 1);
      STAMP(4 + kc);
    }
    if (step < 15) rdx(step + 1);
    asm("v_mfma_f32_16x16x32_bf16 %0, %2, %3, %0\n\tv_mfma_f32_16x16x32_bf16 %1, %2, %2, %1"
        : "+v"(d1), "+v"(d2)
        : "v"(xa[ks][wn]), "v"(ones));
    __builtin_amdgcn_sched_barrier(0);
#pragma unroll
    for (int bb = 0; bb < NBW; ++bb) {
      const int b = wn * NB0 + bb;
      if (b < NAB) {
        asm("v_mfma_f32_16x16x32_bf16 %0, %3, %5, %0\n\tv_mfma_f32_16x16x32_bf16 %1, %4, %5, %1\n\t"
            "v_mfma_f32_16x16x32_bf16 %2, %6, %5, %2"
            : "+v"(acc[0][bb]), "+v"(acc[1][bb]), "+v"(cs[bb])
            : "v"(xa[ks][0]), "v"(xa[ks][1]), "v"(wf[bb]), "v"(ones));
      } else {
        asm("v_mfma_f32_16x16x32_bf16 %0, %5, %3, %0\n\tv_mfma_f32_16x16x32_bf16 %1, %5, %4, %1\n\t"
            "v_mfma_f32_16x16x32_bf16 %2, %5, %6, %2"
            : "+v"(acc[0][bb]), "+v"(acc[1][bb]), "+v"(cs[bb])
            : "v"(xa[ks][0]), "v"(xa[ks][1]), "v"(wf[bb]), "v"(ones));
      }
      if (step < 15) rdw(step + 1, bb);
      __builtin_amdgcn_sched_barrier(0);
    }
  }
  STAMP(12);

  {
    asm volatile("s_nop 7" : "+v"(d1), "+v"(d2));
    const bool valid = q4 == g;
    const int rr = li & 3;
    const float s = rr == 0 ? d1[0] : rr == 1 ? d1[1] : rr == 2 ? d1[2] : d1[3];
    const float q = rr == 0 ? d2[0] : rr == 1 ? d2[1] : rr == 2 ? d2[2] : d2[3];
    float mean = amul(s, 1.0f / 512.0f);
    float varf = afma(q, 1.0f / 512.0f, -mean * mean);
    const int srow = 8 * q4 + 4 * wn + rr;
    if (__any(valid && amul(mean, mean) > amul(8.0f, varf))) {
      mean = __shfl(mean, q4 * 16 + li);
      float v0 = 0.f, v1 = 0.f;
      for (int ks = 0; ks < 16; ++ks) {
        const u32x4 v = *(const u32x4*)(x + (m0 + srow) * DM + ks * 32 + g * 8);
#pragma unroll
        for (int qq = 0; qq < 4; ++qq) {
          const float e0 = aadd(bf_lo(v[qq]), -mean), e1 = aadd(bf_hi(v[qq]), -mean);
          v0 = afma(e0, e0, v0);
          v1 = afma(e1, e1, v1);
        }
      }
      float vs = v0 + v1;
      vs += __shfl_xor(vs, 16);
      vs += __shfl_xor(vs, 32);
      varf = vs * (1.0f / 512.0f);
    }
    const float var = rbf(varf);
    const float sd = rbf(sqrtf(rbf(var + 1e-5f)));
    const float rinv = 1.0f / sd;
    const float mb16 = rbf(mean);
    if (valid) ((float2*)(smem + K1_STATS))[wm * 32 + srow] = make_float2(rinv, -mb16 * rinv);
    if (NH > 0 && tid < NH * 4) *(u32x2*)(smem + K1_BIAS + tid * 8) = bias_v;
  }
  WAIT_L0();
  BAR();

  const __amdgpu_buffer_rsrc_t rsB = __builtin_amdgcn_make_buffer_rsrc((void*)BT, 0, DM * KTOT * 2, 0x00020000);
  const float2* stp = (const float2*)(smem + K1_STATS) + wm * 32;
  float2 sta[8];
  float2 sth[2];
  if (NAB > wn * NB0) {
#pragma unroll
    for (int i = 0; i < 8; ++i) sta[i] = stp[8 * g + i];
  }
  if (NAB < 11 && !(wn == 0 && NAB >= NB0)) {
#pragma unroll
    for (int mb = 0; mb < 2; ++mb) sth[mb] = stp[8 * q4 + 4 * mb + (li & 3)];
  }
#pragma unroll
  for (int bb = 0; bb < NBW; ++bb) {
    const int b = wn * NB0 + bb;
    if (b < NAB) {
      const int n = c0 + 16 * b + li;
      const bool isb = n >= 512;
      const unsigned sgn = isb ? 0x80008000u : 0u;
      u32x4 o;
#pragma unroll
      for (int mb = 0; mb < 2; ++mb) {
        float t0, t1, t2, t3;
        unsigned o0, o1;
        asm("v_mul_f32 %2, %11, %10\n\tv_mul_f32 %3, %13, %10\n\tv_mul_f32 %4, %15, %10\n\tv_mul_f32 %5, %17, %10\n\t"
            "v_fma_f32 %2, %12, %6, %2\n\tv_fma_f32 %3, %14, %7, %3\n\tv_fma_f32 %4, %16, %8, %4\n\tv_fma_f32 %5, %18, %9, %5\n\t"
            "v_cvt_pk_bf16_f32 %0, %2, %3\n\tv_cvt_pk_bf16_f32 %1, %4, %5\n\t"
            "v_xor_b32 %0, %19, %0\n\tv_xor_b32 %1, %19, %1"
            : "=&v"(o0), "=&v"(o1), "=&v"(t0), "=&v"(t1), "=&v"(t2), "=&v"(t3)
            : "v"(acc[mb][bb][0]), "v"(acc[mb][bb][1]), "v"(acc[mb][bb][2]), "v"(acc[mb][bb][3]), "v"(cs[bb][0]),
              "v"(sta[4 * mb + 0].y), "v"(sta[4 * mb + 0].x), "v"(sta[4 * mb + 1].y), "v"(sta[4 * mb + 1].x),
              "v"(sta[4 * mb + 2].y), "v"(sta[4 * mb + 2].x), "v"(sta[4 * mb + 3].y), "v"(sta[4 * mb + 3].x),
              "v"(sgn));
        o[2 * mb] = o0;
        o[2 * mb + 1] = o1;
      }
      __builtin_amdgcn_raw_buffer_store_b128(o, rsB, ((((n & 511) >> 4) * (KTOT / 32) + (((isb ? 2048 : 0) + m0) >> 5)) * 64 + lane) * 16, 0, 0);
    } else {
      const int fl = 16 * (b - NAB) + 4 * g;
      const u32x2 bv = *(const u32x2*)(smem + K1_BIAS + fl * 2);
      const float bias0 = bf_lo(bv[0]), bias1 = bf_hi(bv[0]), bias2 = bf_lo(bv[1]), bias3 = bf_hi(bv[1]);
#pragma unroll
      for (int mb = 0; mb < 2; ++mb) {
        float t0, t1, t2, t3;
        unsigned o0, o1;
        asm("v_fma_f32 %2, %19, %10, %14\n\tv_fma_f32 %3, %19, %11, %15\n\tv_fma_f32 %4, %19, %12, %16\n\tv_fma_f32 %5, %19, %13, %17\n\t"
            "v_fma_f32 %2, %18, %6, %2\n\tv_fma_f32 %3, %18, %7, %3\n\tv_fma_f32 %4, %18, %8, %4\n\tv_fma_f32 %5, %18, %9, %5\n\t"
            "v_max_f32 %2, 0, %2\n\tv_max_f32 %3, 0, %3\n\tv_max_f32 %4, 0, %4\n\tv_max_f32 %5, 0, %5\n\t"
            "v_mul_f32 %2, %2, %2\n\tv_mul_f32 %3, %3, %3\n\tv_mul_f32 %4, %4, %4\n\tv_mul_f32 %5, %5, %5\n\t"
            "v_cvt_pk_bf16_f32 %0, %2, %3\n\tv_cvt_pk_bf16_f32 %1, %4, %5"
            : "=&v"(o0), "=&v"(o1), "=&v"(t0), "=&v"(t1), "=&v"(t2), "=&v"(t3)
            : "v"(acc[mb][bb][0]), "v"(acc[mb][bb][1]), "v"(acc[mb][bb][2]), "v"(acc[mb][bb][3]),
              "v"(cs[bb][0]), "v"(cs[bb][1]), "v"(cs[bb][2]), "v"(cs[bb][3]),
              "v"(bias0), "v"(bias1), "v"(bias2), "v"(bias3), "v"(sth[mb].x), "v"(sth[mb].y));
        u32x2 o;
        o[0] = o0;
        o[1] = o1;
        const int row = wm * 32 + 8 * q4 + 4 * mb + (li & 3);
        *(u32x2*)(smem + row * (NHD * 32) + fl * 2) = o;
      }
    }
  }
  if (NH > 0) {
    WAIT_L0();
    BAR();
    const __amdgpu_buffer_rsrc_t rsH = __builtin_amdgcn_make_buffer_rsrc((void*)H, 0, SEQ * FF * 2, 0x00020000);
    const int hoff = ((mt * 128) * FF + (c0 + 16 * NAB - 1024)) * 2;
#pragma unroll
    for (int i = 0; i < (128 * NHD * 2 + 511) / 512; ++i) {
      const unsigned id = tid + 512 * i;
      if (128 * NHD * 2 % 512 == 0 || id < 128 * NHD * 2) {
        const unsigned row = id / (NHD * 2), c = id - row * (NHD * 2);
        __builtin_amdgcn_raw_buffer_store_b128(*(const u32x4*)(smem + id * 16), rsH, hoff + (row * FF + c * 8) * 2, 0, 0);
      }
    }
  }
  asm volatile("" ::"v"(pf0), "v"(pf1));
}

__global__ __launch_bounds__(512, 2) void k1(const u16* __restrict__ x, const u16* __restrict__ Wr,
                                             const u16* __restrict__ Wi, const u16* __restrict__ w1,
                                             const u16* __restrict__ b1, const u16* __restrict__ w2,
                                             u16* __restrict__ BT, u16* __restrict__ H,
                                             unsigned* __restrict__ cnt, const u16* __restrict__ Cr,
                                             const u16* __restrict__ Ci) {
  extern __shared__ __attribute__((aligned(16))) char smem[];
  const int bid = blockIdx.x;
  const int xcd = bid & 7, idx = bid >> 3;
  const int mt = (xcd & 1) * 8 + (idx >> 2);
  const int nt = (xcd >> 1) * 4 + (idx & 3);
  const int tid = threadIdx.x;

  RSTAMP(0, 30);
  const int tf0 = (bid >> 3) * 64, te0 = (bid & 7) * 64;
  u32x4 w2v = {0u, 0u, 0u, 0u};
  if (bid < 224) w2v = *(const u32x4*)(w2 + (tf0 + (tid >> 3)) * DM + te0 + (tid & 7) * 8);
  if (bid == 0 && tid < 64) cnt[tid * 64] = 0u;

  const char* pf = (const char*)((bid < 128) ? Cr : Ci) + (size_t)(bid & 127) * 65536 + tid * 64;
  const int wn = __builtin_amdgcn_readfirstlane(tid >> 8);
  if (wn == 0) {
    if (nt <= 4) k1_body<11, 0>(x, Wr, Wi, w1, b1, BT, H, smem, mt, nt, pf);
    else if (nt == 5) k1_body<9, 0>(x, Wr, Wi, w1, b1, BT, H, smem, mt, nt, pf);
    else k1_body<0, 0>(x, Wr, Wi, w1, b1, BT, H, smem, mt, nt, pf);
  } else {
    if (nt <= 4) k1_body<11, 1>(x, Wr, Wi, w1, b1, BT, H, smem, mt, nt, pf);
    else if (nt == 5) k1_body<9, 1>(x, Wr, Wi, w1, b1, BT, H, smem, mt, nt, pf);
    else k1_body<0, 1>(x, Wr, Wi, w1, b1, BT, H, smem, mt, nt, pf);
  }
  STAMP(13);
  if (bid < 224) {
    u16* T = (u16*)(smem + 65536);
    const int f = tid >> 3, e8 = (tid & 7) * 8;
#pragma unroll
    for (int q = 0; q < 4; ++q) {
      T[(e8 + 2 * q) * 72 + f] = (u16)(w2v[q] & 0xffffu);
      T[(e8 + 2 * q + 1) * 72 + f] = (u16)(w2v[q] >> 16);
    }
    __syncthreads();
    const int e = tid >> 3, f8 = (tid & 7) * 8;
    const u32x4 v = *(const u32x4*)(T + e * 72 + f8);
    const int n = te0 + e, k = 4096 + tf0 + f8;
    *(u32x4*)((char*)BT + ((((n >> 4) * (KTOT / 32) + (k >> 5)) * 64 + ((k >> 3) & 3) * 16 + (n & 15)) * 16)) = v;
  }
  RSTAMP(0, 31);
}

#define K2_NBUF 4
#define K2_TILE 16384
#define K2_LDS 65536
#define K2_NT 23
#define K2_ACLOB "a0", "a1", "a2", "a3", "a4", "a5", "a6", "a7", "a8", "a9", "a10", "a11", "a12", "a13", "a14", "a15", \
                 "a16", "a17", "a18", "a19", "a20", "a21", "a22", "a23", "a24", "a25", "a26", "a27", "a28", "a29", "a30", "a31"

template <int SK>
__device__ __forceinline__ void k2_epi(f32x4 (&r)[4][2], float* __restrict__ slabs, unsigned* __restrict__ cnt,
                                       const u16* __restrict__ x, const u16* __restrict__ b2,
                                       u16* __restrict__ out, int tile, int row0, int col0, int w,
                                       const u32x2 (&xv)[2], const u32x2 (&bv)[2]) {
  const int tid = threadIdx.x, lane = tid & 63;
  const int li = lane & 15, g = lane >> 4;
  const __amdgpu_buffer_rsrc_t rs = __builtin_amdgcn_make_buffer_rsrc((void*)slabs, 0, 64 * 16 * 8192, 0x00020000);
#pragma unroll
  for (int d = 0; d < 4; ++d) {
    if (d == SK) continue;
    const f32x4 p0 = r[d][0], p1 = r[d][1];
    u32x4 o;
    o[0] = pack2(p0[0], p0[1]);
    o[1] = pack2(p0[2], p0[3]);
    o[2] = pack2(p1[0], p1[1]);
    o[3] = pack2(p1[2], p1[3]);
    __builtin_amdgcn_raw_buffer_store_b128(o, rs, ((tile * 4 + d) * 4 + SK) * 8192 + w * 1024 + lane * 16, 0, 16);
  }
  const int row = row0 + SK * 16 + li;
  const int colb = col0 + 4 * g;
  WAIT_V(0);
  STAMP2(6);
  __syncthreads();
  if (tid == 0) {
    unsigned* c = cnt + tile * 64;
    __hip_atomic_fetch_add(c, 1u, __ATOMIC_RELAXED, __HIP_MEMORY_SCOPE_AGENT);
    unsigned spins = 0;
    while (__hip_atomic_load(c, __ATOMIC_RELAXED, __HIP_MEMORY_SCOPE_AGENT) < 4u) {
      __builtin_amdgcn_s_sleep(1);
      if (++spins > (1u << 24)) break;
    }
  }
  __syncthreads();
  STAMP2(7);
  u32x4 part[4];
#pragma unroll
  for (int s = 0; s < 4; ++s)
    if (s != SK) part[s] = __builtin_amdgcn_raw_buffer_load_b128(rs, ((tile * 4 + SK) * 4 + s) * 8192 + w * 1024 + lane * 16, 0, 16);
#pragma unroll
  for (int j = 0; j < 2; ++j) {
    float sum[4];
#pragma unroll
    for (int i = 0; i < 4; ++i) {
      float t = r[SK][j][i];
#pragma unroll
      for (int s = 0; s < 4; ++s)
        if (s != SK) t = aadd(t, (i & 1) ? bf_hi(part[s][2 * j + (i >> 1)]) : bf_lo(part[s][2 * j + (i >> 1)]));
      sum[i] = t;
    }
    u32x2 o;
    o[0] = pack2(aadd(aadd(bf_lo(xv[j][0]), sum[0]), bf_lo(bv[j][0])), aadd(aadd(bf_hi(xv[j][0]), sum[1]), bf_hi(bv[j][0])));
    o[1] = pack2(aadd(aadd(bf_lo(xv[j][1]), sum[2]), bf_lo(bv[j][1])), aadd(aadd(bf_hi(xv[j][1]), sum[3]), bf_hi(bv[j][1])));
    *(u32x2*)(out + row * DM + colb + j * 16) = o;
  }
  STAMP2(8);
}

__global__ __launch_bounds__(512, 2) void k2(const u16* __restrict__ Cr, const u16* __restrict__ Ci,
                                             const u16* __restrict__ H, const u16* __restrict__ BT,
                                             const u16* __restrict__ x, const u16* __restrict__ b2,
                                             u16* __restrict__ out, float* __restrict__ slabs,
                                             unsigned* __restrict__ cnt) {
  extern __shared__ __attribute__((aligned(16))) char smem[];
  const int bid = blockIdx.x;
  const int xcd = bid & 7, idx = bid >> 3;
  const int sk = xcd >> 1;
  const int mt = (xcd & 1) * 8 + (idx >> 2);
  const int nt = idx & 3;
  const int tid = threadIdx.x, lane = tid & 63;
  const int w = __builtin_amdgcn_readfirstlane(tid >> 6);
  const int ksel = w >> 2, wn = w & 3;
  const int li = lane & 15, g = lane >> 4;
  const int m0 = mt * 128, n0 = nt * 128;

  RSTAMP(1, 30);
  const int row0 = 16 * w + (lane >> 3);
  const int chE = ((lane & 7) ^ (lane >> 4)) * 8, chO = chE ^ 32;
  const int kt0 = sk * K2_NT;
  const char* bsrc = (const char*)BT + ((((n0 >> 4) + 2 * wn) * (KTOT / 32) + 2 * kt0 + ksel) * 64 + lane) * 16;

  f32x4 acc[8][2];
#pragma unroll
  for (int i = 0; i < 8; ++i)
#pragma unroll
    for (int j = 0; j < 2; ++j) acc[i][j] = (f32x4){0.f, 0.f, 0.f, 0.f};
  const int rdA = li * 128 + ((4 * ksel + g) ^ (li >> 1)) * 16;
  bf16x8 af[8];

#define K2_ISSUE(T, S)                                                                                         \
  do {                                                                                                         \
    const int kt_ = kt0 + (T);                                                                                 \
    char* dst_ = smem + (S) * K2_TILE + w * 2048;                                                              \
    const u16* ap_ = (kt_ < 32) ? (Cr + kt_ * 64) : (kt_ < 64) ? (Ci + (kt_ - 32) * 64) : (H + (kt_ - 64) * 64); \
    const int pitch_ = (kt_ < 64) ? 2048 : FF;                                                                 \
    const u16* asrc_ = ap_ + (m0 + row0) * pitch_;                                                             \
    glds16(asrc_ + chE, dst_);                                                                                 \
    glds16(asrc_ + 8 * pitch_ + chO, dst_ + 1024);                                                             \
    const char* bp_ = bsrc + (T) * 2048;                                                                       \
    asm volatile("global_load_dwordx4 a[%c2:%c3], %0, off\n\tglobal_load_dwordx4 a[%c4:%c5], %1, off"          \
                 :                                                                                             \
                 : "v"(bp_), "v"(bp_ + (KTOT / 32) * 1024), "i"(8 * (S)), "i"(8 * (S) + 3), "i"(8 * (S) + 4),  \
                   "i"(8 * (S) + 7)                                                                            \
                 : "memory", K2_ACLOB);                                                                        \
  } while (0)
#define K2_RD(S)                                                                                               \
  do {                                                                                                         \
    _Pragma("unroll") for (int i_ = 0; i_ < 8; ++i_) af[i_] = *(const bf16x8*)(smem + (S) * K2_TILE + rdA + i_ * 2048); \
  } while (0)
#define K2_MM4(S, I)                                                                                           \
  asm volatile("s_nop 1\n\tv_mfma_f32_16x16x32_bf16 %0, a[%c12:%c13], %8, %0\n\tv_mfma_f32_16x16x32_bf16 %1, a[%c14:%c15], %8, %1\n\t" \
      "v_mfma_f32_16x16x32_bf16 %2, a[%c12:%c13], %9, %2\n\tv_mfma_f32_16x16x32_bf16 %3, a[%c14:%c15], %9, %3\n\t"  \
      "v_mfma_f32_16x16x32_bf16 %4, a[%c12:%c13], %10, %4\n\tv_mfma_f32_16x16x32_bf16 %5, a[%c14:%c15], %10, %5\n\t" \
      "v_mfma_f32_16x16x32_bf16 %6, a[%c12:%c13], %11, %6\n\tv_mfma_f32_16x16x32_bf16 %7, a[%c14:%c15], %11, %7"     \
      : "+a"(acc[(I)][0]), "+a"(acc[(I)][1]), "+a"(acc[(I) + 1][0]), "+a"(acc[(I) + 1][1]), "+a"(acc[(I) + 2][0]),  \
        "+a"(acc[(I) + 2][1]), "+a"(acc[(I) + 3][0]), "+a"(acc[(I) + 3][1])                                    \
      : "v"(af[(I)]), "v"(af[(I) + 1]), "v"(af[(I) + 2]), "v"(af[(I) + 3]), "i"(8 * (S)), "i"(8 * (S) + 3),    \
        "i"(8 * (S) + 4), "i"(8 * (S) + 7))
#define K2_MM(S) do { K2_MM4(S, 0); K2_MM4(S, 4); } while (0)
#define K2_STEP_A(T, S, NV, ISS)                    \
  do {                                              \
    wait_vm<NV>();                                  \
    WAIT_L0();                                      \
    BAR();                                          \
    if (ISS) K2_ISSUE((T) + 3, ((S) + 3) & 3);      \
    K2_RD(S);                                       \
    K2_MM(S);                                       \
    __builtin_amdgcn_sched_barrier(0);              \
  } while (0)
#define K2_STEP_B(T, S, NV, ISS)                    \
  do {                                              \
    wait_vm<NV>();                                  \
    WAIT_L0();                                      \
    BAR();                                          \
    K2_MM(((S) + 3) & 3);                           \
    __builtin_amdgcn_sched_barrier(0);              \
    if (ISS) K2_ISSUE((T) + 3, ((S) + 3) & 3);      \
    K2_RD(S);                                       \
    __builtin_amdgcn_sched_barrier(0);              \
  } while (0)

  const int rw0 = m0 + ksel * 64, cl0 = n0 + wn * 32;
  u32x2 xv[2], bv[2];
#pragma unroll
  for (int j = 0; j < 2; ++j) {
    xv[j] = *(const u32x2*)(x + (rw0 + sk * 16 + li) * DM + cl0 + 4 * g + j * 16);
    bv[j] = *(const u32x2*)(b2 + cl0 + 4 * g + j * 16);
  }
#pragma unroll
  for (int i = 0; i < 8; ++i) asm volatile("s_nop 1" : "+a"(acc[i][0]), "+a"(acc[i][1]));
  STAMP2(0);
  K2_ISSUE(0, 0);
  K2_ISSUE(1, 1);
  K2_ISSUE(2, 2);
  if (w < 4) {
#pragma unroll 1
    for (int t = 0; t < 20; t += 4) {
      K2_STEP_A(t, 0, 8, true);
      K2_STEP_A(t + 1, 1, 8, true);
      K2_STEP_A(t + 2, 2, 8, true);
      K2_STEP_A(t + 3, 3, 8, true);
    }
    K2_STEP_A(20, 0, 8, false);
    K2_STEP_A(21, 1, 4, false);
    K2_STEP_A(22, 2, 0, false);
  } else {
    wait_vm<8>();
    BAR();
    K2_ISSUE(3, 3);
    K2_RD(0);
    K2_STEP_B(1, 1, 8, true);
    K2_STEP_B(2, 2, 8, true);
    K2_STEP_B(3, 3, 8, true);
#pragma unroll 1
    for (int t = 4; t < 20; t += 4) {
      K2_STEP_B(t, 0, 8, true);
      K2_STEP_B(t + 1, 1, 8, true);
      K2_STEP_B(t + 2, 2, 8, true);
      K2_STEP_B(t + 3, 3, 8, true);
    }
    K2_STEP_B(20, 0, 8, false);
    K2_STEP_B(21, 1, 4, false);
    K2_STEP_B(22, 2, 0, false);
    K2_MM(2);
  }
  STAMP2(5);
  f32x4 r[4][2];
#pragma unroll
  for (int i = 0; i < 8; ++i) asm volatile("s_nop 7" : "+a"(acc[i][0]), "+a"(acc[i][1]));
  WAIT_L0();
  BAR();
  {
    char* mine = smem + w * 8192 + lane * 16;
    const char* theirs = smem + (w ^ 4) * 8192 + lane * 16;
    if (ksel == 0) {
#pragma unroll
      for (int i = 0; i < 4; ++i)
#pragma unroll
        for (int j = 0; j < 2; ++j) { *(f32x4*)(mine + (i * 2 + j) * 1024) = acc[4 + i][j]; r[i][j] = acc[i][j]; }
    } else {
#pragma unroll
      for (int i = 0; i < 4; ++i)
#pragma unroll
        for (int j = 0; j < 2; ++j) { *(f32x4*)(mine + (i * 2 + j) * 1024) = acc[i][j]; r[i][j] = acc[4 + i][j]; }
    }
    WAIT_L0();
    BAR();
    f32x4 o[4][2];
#pragma unroll
    for (int i = 0; i < 4; ++i)
#pragma unroll
      for (int j = 0; j < 2; ++j) o[i][j] = *(const f32x4*)(theirs + (i * 2 + j) * 1024);
#pragma unroll
    for (int i = 0; i < 4; ++i)
#pragma unroll
      for (int j = 0; j < 2; ++j)
#pragma unroll
        for (int q = 0; q < 4; ++q) r[i][j][q] = aadd(r[i][j][q], o[i][j][q]);
  }
  const int tile = mt * 4 + nt;
  if (sk == 0) k2_epi<0>(r, slabs, cnt, x, b2, out, tile, rw0, cl0, w, xv, bv);
  else if (sk == 1) k2_epi<1>(r, slabs, cnt, x, b2, out, tile, rw0, cl0, w, xv, bv);
  else if (sk == 2) k2_epi<2>(r, slabs, cnt, x, b2, out, tile, rw0, cl0, w, xv, bv);
  else k2_epi<3>(r, slabs, cnt, x, b2, out, tile, rw0, cl0, w, xv, bv);
  RSTAMP(1, 31);
}

extern "C" void kernel_launch(void* const* d_in, const int* in_sizes, int n_in, void* d_out, int out_size,
                              void* d_ws, size_t ws_size, hipStream_t stream) {
  const u16* x = (const u16*)d_in[0];
  const u16* Cr = (const u16*)d_in[1];
  const u16* Ci = (const u16*)d_in[2];
  const u16* Wr = (const u16*)d_in[3];
  const u16* Wi = (const u16*)d_in[4];
  const u16* w1 = (const u16*)d_in[6];
  const u16* b1 = (const u16*)d_in[7];
  const u16* w2 = (const u16*)d_in[8];
  const u16* b2 = (const u16*)d_in[9];
  u16* out = (u16*)d_out;
  char* ws = (char*)d_ws;
  u16* BT = (u16*)ws;
  u16* H = (u16*)(ws + 6029312);
  float* slabs = (float*)(ws + 6029312 + 7340032);
  unsigned* cnt = (unsigned*)(ws + 6029312 + 7340032 + 16777216);

  (void)hipFuncSetAttribute((const void*)k1, hipFuncAttributeMaxDynamicSharedMemorySize, K1_LDS);
  (void)hipFuncSetAttribute((const void*)k2, hipFuncAttributeMaxDynamicSharedMemorySize, K2_LDS);
  k1<<<256, 512, K1_LDS, stream>>>(x, Wr, Wi, w1, b1, w2, BT, H, cnt, Cr, Ci);
  k2<<<256, 512, K2_LDS, stream>>>(Cr, Ci, H, BT, x, b2, out, slabs, cnt);
}
```

```cpp
#include <hip/hip_runtime.h>
#include <hip/hip_bf16.h>
#include <stdint.h>

typedef unsigned short u16;
typedef __attribute__((ext_vector_type(8))) short bf16x8;
typedef __attribute__((ext_vector_type(4))) short bf16x4;
typedef __attribute__((ext_vector_type(2))) short bf16x2;
typedef __attribute__((ext_vector_type(4))) float f32x4;
typedef __attribute__((ext_vector_type(4))) unsigned int u32x4;
typedef __attribute__((ext_vector_type(2))) unsigned int u32x2;

#define SEQ 2048
#define DM 512
#define FF 1792
#define KTOT 5888

#define STAMP(i)
#define STAMP2(i)
#define RSTAMP(k, i)
#define WAIT_V(n) asm volatile("s_waitcnt vmcnt(" #n ")" ::: "memory")
#define WAIT_L0() asm volatile("s_waitcnt lgkmcnt(0)" ::: "memory")
#define BAR() asm volatile("s_barrier" ::: "memory")

__device__ __forceinline__ float bf2f(u16 v) { return __uint_as_float(((unsigned)v) << 16); }
__device__ __forceinline__ float bf_lo(unsigned v) { return __uint_as_float(v << 16); }
__device__ __forceinline__ float bf_hi(unsigned v) { return __uint_as_float(v & 0xffff0000u); }
__device__ __forceinline__ unsigned pack2(float lo, float hi) {
  unsigned r;
  asm("v_cvt_pk_bf16_f32 %0, %1, %2" : "=v"(r) : "v"(lo), "v"(hi));
  return r;
}
__device__ __forceinline__ float rbf(float f) { return bf_lo(pack2(f, f)); }
__device__ __forceinline__ float afma(float a, float b, float c) {
  float r;
  asm("v_fma_f32 %0, %1, %2, %3" : "=v"(r) : "v"(a), "v"(b), "v"(c));
  return r;
}
__device__ __forceinline__ float aadd(float a, float b) {
  float r;
  asm("v_add_f32 %0, %1, %2" : "=v"(r) : "v"(a), "v"(b));
  return r;
}
__device__ __forceinline__ float amul(float a, float b) {
  float r;
  asm("v_mul_f32 %0, %1, %2" : "=v"(r) : "v"(a), "v"(b));
  return r;
}

template <int N> __device__ __forceinline__ void wait_vm() {
  asm volatile("s_waitcnt vmcnt(%0)" ::"i"(N) : "memory");
}

__device__ __forceinline__ void glds16(const void* g, void* l) {
  __builtin_amdgcn_global_load_lds((const __attribute__((address_space(1))) unsigned*)g,
                                   (__attribute__((address_space(3))) unsigned*)l, 16, 0, 0);
}

#define PF_A 2
#define PF_B 5
__device__ constexpr int k1_allowed(int kc) {
  return (kc + 2 <= 7 ? 5 : 0) + ((PF_A >= kc - 2 && PF_A <= kc - 1) ? 1 : 0) + ((PF_B >= kc - 2 && PF_B <= kc - 1) ? 1 : 0);
}
#define K1_CHUNK 38912
#define K1_NBUF 4
#define K1_STATS (K1_NBUF * K1_CHUNK)
#define K1_BIAS (K1_STATS + 1024)
#define K1_LDS (K1_BIAS + 512)

template <int NAB, int WN>
__device__ __forceinline__ void k1_body(const u16* __restrict__ x, const u16* __restrict__ Wr,
                                        const u16* __restrict__ Wi, const u16* __restrict__ w1,
                                        const u16* __restrict__ b1, u16* __restrict__ BT,
                                        u16* __restrict__ H, char* smem, int mt, int nt,
                                        const char* pf) {
  constexpr int NH = 11 - NAB;
  constexpr int ROT = (NH == 11) ? 4 : 1;
  constexpr int NHD = (NH == 0) ? 1 : NH;
  constexpr int NB0 = 6;
  constexpr int wn = WN;
  constexpr int NBW = (wn == 0) ? 6 : 5;
  const int tid = threadIdx.x;
  const int lane = tid & 63;
  const int w = __builtin_amdgcn_readfirstlane(tid >> 6);
  const int wm = w & 3;
  const int li = lane & 15, g = lane >> 4;
  const int c0 = nt * 176;
  const int m0 = mt * 128 + wm * 32;

  STAMP(0);
  const u16* src[5];
  int inc[5], jd[5];
#pragma unroll
  for (int jj = 0; jj < 2; ++jj) {
    const int row = 16 * w + 8 * jj + (lane >> 3);
    const int sw = (((row >> 3) & 3) << 1) | ((row >> 1) & 1);
    src[jj] = x + (mt * 128 + row) * DM + ((lane & 7) ^ sw) * 8;
    inc[jj] = 64;
    jd[jj] = (2 * w + jj) * 1024;
  }
#pragma unroll
  for (int jj = 0; jj < 3; ++jj) {
    int j = w + 8 * jj;
    if (j > 21) j -= 8;
    jd[2 + jj] = 16384 + j * 1024;
    if (NAB == 11 || (NAB > 0 && j < 2 * NAB)) {
      const int row = 8 * j + (lane >> 3);
      const int ch = (lane & 7) ^ ((row >> 1) & 7);
      const int n = c0 + row;
      const u16* base = (n < 512) ? (Wr + n * DM) : (Wi + (n - 512) * DM);
      src[2 + jj] = base + ch * 8;
      inc[2 + jj] = 64;
    } else {
      const unsigned p16 = (j - 2 * NAB) * 64 + lane;
      const unsigned r = p16 / (2 * NHD);
      const unsigned rem = p16 - r * (2 * NHD);
      const int cp = rem >> 1, hf = rem & 1;
      int c = cp - ROT * ((r >> 3) & 1);
      if (c < 0) c += NH;
      src[2 + jj] = w1 + r * FF + (c0 + 16 * NAB - 1024) + 16 * c + 8 * hf;
      inc[2 + jj] = 64 * FF;
    }
  }
  auto issue = [&](int kc) {
#pragma unroll
    for (int jj = 0; jj < 5; ++jj) glds16(src[jj] + kc * inc[jj], smem + (kc % K1_NBUF) * K1_CHUNK + jd[jj]);
  };
  u32x2 bias_v = {0u, 0u};
  if (NH > 0 && tid < NH * 4) bias_v = *(const u32x2*)(b1 + (c0 + 16 * NAB - 1024) + 4 * tid);
  issue(0);
  issue(1);
  issue(2);

  f32x4 acc[2][NBW], cs[NBW];
#pragma unroll
  for (int b = 0; b < NBW; ++b) {
    acc[0][b] = (f32x4){0.f, 0.f, 0.f, 0.f};
    acc[1][b] = (f32x4){0.f, 0.f, 0.f, 0.f};
    cs[b] = (f32x4){0.f, 0.f, 0.f, 0.f};
  }
  f32x4 d1 = {0.f, 0.f, 0.f, 0.f}, d2 = {0.f, 0.f, 0.f, 0.f};
  bf16x8 ones = {0x3f80, 0x3f80, 0x3f80, 0x3f80, 0x3f80, 0x3f80, 0x3f80, 0x3f80};
  asm volatile("" : "+v"(ones));

  const int q4 = li >> 2, p4 = lane & 3;
  const int laneX0 = (wm * 32 + 8 * q4 + (li & 3)) * 128 + ((0 + g) ^ (li >> 1)) * 16;
  const int laneX1 = (wm * 32 + 8 * q4 + (li & 3)) * 128 + ((4 + g) ^ (li >> 1)) * 16;
  const int laneAB0 = 16384 + li * 128 + ((0 + g) ^ (li >> 1)) * 16;
  const int laneAB1 = 16384 + li * 128 + ((4 + g) ^ (li >> 1)) * 16;
  const int laneH0 = 16384 + NAB * 2048 + (8 * g + q4) * (NHD * 32) + (g & 1) * ROT * 32 + p4 * 8;
  const int laneH1 = laneH0 - (g & 1) * NH * 32;
  int boff = 0;
  unsigned pf0 = 0, pf1 = 0;
  auto setbuf = [&](int kc) {
    boff = (kc % K1_NBUF) * K1_CHUNK;
    asm volatile("" : "+s"(boff));
  };
  bf16x8 wf[NBW], xa[2][2];
  auto rdx = [&](int step) {
    const int ks = step & 1;
    xa[ks][0] = *(const bf16x8*)(smem + boff + (ks ? laneX1 : laneX0));
    xa[ks][1] = *(const bf16x8*)(smem + boff + (ks ? laneX1 : laneX0) + 512);
  };
  auto rdw = [&](int step, int bb) {
    const int ks = step & 1;
    const int b = wn * NB0 + bb;
    if (b < NAB) {
      wf[bb] = *(const bf16x8*)(smem + boff + (ks ? laneAB1 : laneAB0) + b * 2048);
    } else {
      const int hb = b - NAB;
      const char* hp = smem + boff + ((hb + ROT >= NH) ? laneH1 : laneH0) + 32 * ks * (NHD * 32) + hb * 32;
      const bf16x4 t0 = __builtin_amdgcn_ds_read_tr16_b64_v4i16((__attribute__((address_space(3))) bf16x4*)(hp));
      const bf16x4 t1 = __builtin_amdgcn_ds_read_tr16_b64_v4i16((__attribute__((address_space(3))) bf16x4*)(hp + 4 * (NHD * 32)));
      wf[bb] = __builtin_shufflevector(t0, t1, 0, 1, 2, 3, 4, 5, 6, 7);
    }
  };
#pragma unroll
  for (int bb = 0; bb < NBW; ++bb) asm volatile("s_nop 1" : "+v"(acc[0][bb]), "+v"(acc[1][bb]), "+v"(cs[bb]));
  asm volatile("s_nop 1" : "+v"(d1), "+v"(d2));
  wait_vm<10>();
  BAR();
  STAMP(1);
  setbuf(0);
  rdx(0);
#pragma unroll
  for (int bb = 0; bb < NBW; ++bb) rdw(0, bb);
#pragma unroll
  for (int step = 0; step < 16; ++step) {
    const int kc = step >> 1, ks = step & 1;
    if (ks && kc < 7) {
      if (kc == 0) wait_vm<k1_allowed(0)>(); else if (kc == 1) wait_vm<k1_allowed(1)>(); else if (kc == 2) wait_vm<k1_allowed(2)>();
      else if (kc == 3) wait_vm<k1_allowed(3)>(); else if (kc == 4) wait_vm<k1_allowed(4)>(); else if (kc == 5) wait_vm<k1_allowed(5)>();
      else wait_vm<k1_allowed(6)>();
      BAR();
      setbuf(kc + 1);
      STAMP(4 + kc);
    }
    if (step < 15) rdx(step + 1);
    asm("v_mfma_f32_16x16x32_bf16 %0, %2, %3, %0\n\tv_mfma_f32_16x16x32_bf16 %1, %2, %2, %1"
        : "+v"(d1), "+v"(d2)
        : "v"(xa[ks][wn]), "v"(ones));
    __builtin_amdgcn_sched_barrier(0);
#pragma unroll
    for (int bb = 0; bb < NBW; ++bb) {
      const int b = wn * NB0 + bb;
      if (b < NAB) {
        asm("v_mfma_f32_16x16x32_bf16 %0, %3, %5, %0\n\tv_mfma_f32_16x16x32_bf16 %1, %4, %5, %1\n\t"
            "v_mfma_f32_16x16x32_bf16 %2, %6, %5, %2"
            : "+v"(acc[0][bb]), "+v"(acc[1][bb]), "+v"(cs[bb])
            : "v"(xa[ks][0]), "v"(xa[ks][1]), "v"(wf[bb]), "v"(ones));
      } else {
        asm("v_mfma_f32_16x16x32_bf16 %0, %5, %3, %0\n\tv_mfma_f32_16x16x32_bf16 %1, %5, %4, %1\n\t"
            "v_mfma_f32_16x16x32_bf16 %2, %5, %6, %2"
            : "+v"(acc[0][bb]), "+v"(acc[1][bb]), "+v"(cs[bb])
            : "v"(xa[ks][0]), "v"(xa[ks][1]), "v"(wf[bb]), "v"(ones));
      }
      if (step < 15) rdw(step + 1, bb);
      if (ks && kc < 7 && bb < 5) {
        if (kc + 3 < 8) glds16(src[bb] + (kc + 3) * inc[bb], smem + ((kc + 3) % K1_NBUF) * K1_CHUNK + jd[bb]);
        if (bb == 4) {
          asm volatile("" ::: "memory");
          if (kc == PF_A) pf0 = *(const unsigned*)(pf);
          if (kc == PF_B) pf1 = *(const unsigned*)(pf + 32768);
          asm volatile("" ::: "memory");
        }
      }
      __builtin_amdgcn_sched_barrier(0);
    }
  }
  STAMP(12);

  {
    asm volatile("s_nop 7" : "+v"(d1), "+v"(d2));
    const bool valid = q4 == g;
    const int rr = li & 3;
    const float s = rr == 0 ? d1[0] : rr == 1 ? d1[1] : rr == 2 ? d1[2] : d1[3];
    const float q = rr == 0 ? d2[0] : rr == 1 ? d2[1] : rr == 2 ? d2[2] : d2[3];
    float mean = amul(s, 1.0f / 512.0f);
    float varf = afma(q, 1.0f / 512.0f, -mean * mean);
    const int srow = 8 * q4 + 4 * wn + rr;
    if (__any(valid && amul(mean, mean) > amul(8.0f, varf))) {
      mean = __shfl(mean, q4 * 16 + li);
      float v0 = 0.f, v1 = 0.f;
      for (int ks = 0; ks < 16; ++ks) {
        const u32x4 v = *(const u32x4*)(x + (m0 + srow) * DM + ks * 32 + g * 8);
#pragma unroll
        for (int qq = 0; qq < 4; ++qq) {
          const float e0 = aadd(bf_lo(v[qq]), -mean), e1 = aadd(bf_hi(v[qq]), -mean);
          v0 = afma(e0, e0, v0);
          v1 = afma(e1, e1, v1);
        }
      }
      float vs = v0 + v1;
      vs += __shfl_xor(vs, 16);
      vs += __shfl_xor(vs, 32);
      varf = vs * (1.0f / 512.0f);
    }
    const float var = rbf(varf);
    const float sd = rbf(sqrtf(rbf(var + 1e-5f)));
    const float rinv = 1.0f / sd;
    const float mb16 = rbf(mean);
    if (valid) ((float2*)(smem + K1_STATS))[wm * 32 + srow] = make_float2(rinv, -mb16 * rinv);
    if (NH > 0 && tid < NH * 4) *(u32x2*)(smem + K1_BIAS + tid * 8) = bias_v;
  }
  WAIT_L0();
  BAR();

  const __amdgpu_buffer_rsrc_t rsB = __builtin_amdgcn_make_buffer_rsrc((void*)BT, 0, DM * KTOT * 2, 0x00020000);
  const float2* stp = (const float2*)(smem + K1_STATS) + wm * 32;
  float2 sta[8];
  float2 sth[2];
  if (NAB > wn * NB0) {
#pragma unroll
    for (int i = 0; i < 8; ++i) sta[i] = stp[8 * g + i];
  }
  if (NAB < 11 && !(wn == 0 && NAB >= NB0)) {
#pragma unroll
    for (int mb = 0; mb < 2; ++mb) sth[mb] = stp[8 * q4 + 4 * mb + (li & 3)];
  }
#pragma unroll
  for (int bb = 0; bb < NBW; ++bb) {
    const int b = wn * NB0 + bb;
    if (b < NAB) {
      const int n = c0 + 16 * b + li;
      const bool isb = n >= 512;
      const unsigned sgn = isb ? 0x80008000u : 0u;
      u32x4 o;
#pragma unroll
      for (int mb = 0; mb < 2; ++mb) {
        float t0, t1, t2, t3;
        unsigned o0, o1;
        asm("v_mul_f32 %2, %11, %10\n\tv_mul_f32 %3, %13, %10\n\tv_mul_f32 %4, %15, %10\n\tv_mul_f32 %5, %17, %10\n\t"
            "v_fma_f32 %2, %12, %6, %2\n\tv_fma_f32 %3, %14, %7, %3\n\tv_fma_f32 %4, %16, %8, %4\n\tv_fma_f32 %5, %18, %9, %5\n\t"
            "v_cvt_pk_bf16_f32 %0, %2, %3\n\tv_cvt_pk_bf16_f32 %1, %4, %5\n\t"
            "v_xor_b32 %0, %19, %0\n\tv_xor_b32 %1, %19, %1"
            : "=&v"(o0), "=&v"(o1), "=&v"(t0), "=&v"(t1), "=&v"(t2), "=&v"(t3)
            : "v"(acc[mb][bb][0]), "v"(acc[mb][bb][1]), "v"(acc[mb][bb][2]), "v"(acc[mb][bb][3]), "v"(cs[bb][0]),
              "v"(sta[4 * mb + 0].y), "v"(sta[4 * mb + 0].x), "v"(sta[4 * mb + 1].y), "v"(sta[4 * mb + 1].x),
              "v"(sta[4 * mb + 2].y), "v"(sta[4 * mb + 2].x), "v"(sta[4 * mb + 3].y), "v"(sta[4 * mb + 3].x),
              "v"(sgn));
        o[2 * mb] = o0;
        o[2 * mb + 1] = o1;
      }
      __builtin_amdgcn_raw_buffer_store_b128(o, rsB, ((((n & 511) >> 4) * (KTOT / 32) + (((isb ? 2048 : 0) + m0) >> 5)) * 64 + lane) * 16, 0, 16);
    } else {
      const int fl = 16 * (b - NAB) + 4 * g;
      const u32x2 bv = *(const u32x2*)(smem + K1_BIAS + fl * 2);
      const float bias0 = bf_lo(bv[0]), bias1 = bf_hi(bv[0]), bias2 = bf_lo(bv[1]), bias3 = bf_hi(bv[1]);
#pragma unroll
      for (int mb = 0; mb < 2; ++mb) {
        float t0, t1, t2, t3;
        unsigned o0, o1;
        asm("v_fma_f32 %2, %19, %10, %14\n\tv_fma_f32 %3, %19, %11, %15\n\tv_fma_f32 %4, %19, %12, %16\n\tv_fma_f32 %5, %19, %13, %17\n\t"
            "v_fma_f32 %2, %18, %6, %2\n\tv_fma_f32 %3, %18, %7, %3\n\tv_fma_f32 %4, %18, %8, %4\n\tv_fma_f32 %5, %18, %9, %5\n\t"
            "v_max_f32 %2, 0, %2\n\tv_max_f32 %3, 0, %3\n\tv_max_f32 %4, 0, %4\n\tv_max_f32 %5, 0, %5\n\t"
            "v_mul_f32 %2, %2, %2\n\tv_mul_f32 %3, %3, %3\n\tv_mul_f32 %4, %4, %4\n\tv_mul_f32 %5, %5, %5\n\t"
            "v_cvt_pk_bf16_f32 %0, %2, %3\n\tv_cvt_pk_bf16_f32 %1, %4, %5"
            : "=&v"(o0), "=&v"(o1), "=&v"(t0), "=&v"(t1), "=&v"(t2), "=&v"(t3)
            : "v"(acc[mb][bb][0]), "v"(acc[mb][bb][1]), "v"(acc[mb][bb][2]), "v"(acc[mb][bb][3]),
              "v"(cs[bb][0]), "v"(cs[bb][1]), "v"(cs[bb][2]), "v"(cs[bb][3]),
              "v"(bias0), "v"(bias1), "v"(bias2), "v"(bias3), "v"(sth[mb].x), "v"(sth[mb].y));
        u32x2 o;
        o[0] = o0;
        o[1] = o1;
        const int row = wm * 32 + 8 * q4 + 4 * mb + (li & 3);
        *(u32x2*)(smem + row * (NHD * 32) + fl * 2) = o;
      }
    }
  }
  if (NH > 0) {
    WAIT_L0();
    BAR();
    const __amdgpu_buffer_rsrc_t rsH = __builtin_amdgcn_make_buffer_rsrc((void*)H, 0, SEQ * FF * 2, 0x00020000);
    const int hoff = ((mt * 128) * FF + (c0 + 16 * NAB - 1024)) * 2;
#pragma unroll
    for (int i = 0; i < (128 * NHD * 2 + 511) / 512; ++i) {
      const unsigned id = tid + 512 * i;
      if (128 * NHD * 2 % 512 == 0 || id < 128 * NHD * 2) {
        const unsigned row = id / (NHD * 2), c = id - row * (NHD * 2);
        __builtin_amdgcn_raw_buffer_store_b128(*(const u32x4*)(smem + id * 16), rsH, hoff + (row * FF + c * 8) * 2, 0, 16);
      }
    }
  }
  asm volatile("" ::"v"(pf0), "v"(pf1));
}

__global__ __launch_bounds__(512, 2) void k1(const u16* __restrict__ x, const u16* __restrict__ Wr,
                                             const u16* __restrict__ Wi, const u16* __restrict__ w1,
                                             const u16* __restrict__ b1, const u16* __restrict__ w2,
                                             u16* __restrict__ BT, u16* __restrict__ H,
                                             unsigned* __restrict__ cnt, const u16* __restrict__ Cr,
                                             const u16* __restrict__ Ci) {
  extern __shared__ __attribute__((aligned(16))) char smem[];
  const int bid = blockIdx.x;
  const int xcd = bid & 7, idx = bid >> 3;
  const int mt = (xcd & 1) * 8 + (idx >> 2);
  const int nt = (xcd >> 1) * 4 + (idx & 3);
  const int tid = threadIdx.x;

  RSTAMP(0, 30);
  const int tf0 = (bid >> 3) * 64, te0 = (bid & 7) * 64;
  u32x4 w2v = {0u, 0u, 0u, 0u};
  if (bid < 224) w2v = *(const u32x4*)(w2 + (tf0 + (tid >> 3)) * DM + te0 + (tid & 7) * 8);
  if (bid == 0 && tid < 64) cnt[tid * 64] = 0u;

  const char* pf = (const char*)((bid < 128) ? Cr : Ci) + (size_t)(bid & 127) * 65536 + tid * 64;
  const int wn = __builtin_amdgcn_readfirstlane(tid >> 8);
  if (wn == 0) {
    if (nt <= 4) k1_body<11, 0>(x, Wr, Wi, w1, b1, BT, H, smem, mt, nt, pf);
    else if (nt == 5) k1_body<9, 0>(x, Wr, Wi, w1, b1, BT, H, smem, mt, nt, pf);
    else k1_body<0, 0>(x, Wr, Wi, w1, b1, BT, H, smem, mt, nt, pf);
  } else {
    if (nt <= 4) k1_body<11, 1>(x, Wr, Wi, w1, b1, BT, H, smem, mt, nt, pf);
    else if (nt == 5) k1_body<9, 1>(x, Wr, Wi, w1, b1, BT, H, smem, mt, nt, pf);
    else k1_body<0, 1>(x, Wr, Wi, w1, b1, BT, H, smem, mt, nt, pf);
  }
  STAMP(13);
  if (bid < 224) {
    u16* T = (u16*)(smem + 65536);
    const int f = tid >> 3, e8 = (tid & 7) * 8;
#pragma unroll
    for (int q = 0; q < 4; ++q) {
      T[(e8 + 2 * q) * 72 + f] = (u16)(w2v[q] & 0xffffu);
      T[(e8 + 2 * q + 1) * 72 + f] = (u16)(w2v[q] >> 16);
    }
    __syncthreads();
    const int e = tid >> 3, f8 = (tid & 7) * 8;
    const u32x4 v = *(const u32x4*)(T + e * 72 + f8);
    const int n = te0 + e, k = 4096 + tf0 + f8;
    *(u32x4*)((char*)BT + ((((n >> 4) * (KTOT / 32) + (k >> 5)) * 64 + ((k >> 3) & 3) * 16 + (n & 15)) * 16)) = v;
  }
  RSTAMP(0, 31);
}

#define K2_NBUF 4
#define K2_TILE 16384
#define K2_LDS 65536
#define K2_NT 23
#define K2_ACLOB "a0", "a1", "a2", "a3", "a4", "a5", "a6", "a7", "a8", "a9", "a10", "a11", "a12", "a13", "a14", "a15", \
                 "a16", "a17", "a18", "a19", "a20", "a21", "a22", "a23", "a24", "a25", "a26", "a27", "a28", "a29", "a30", "a31"

template <int SK>
__device__ __forceinline__ void k2_epi(f32x4 (&r)[4][2], float* __restrict__ slabs, unsigned* __restrict__ cnt,
                                       const u16* __restrict__ x, const u16* __restrict__ b2,
                                       u16* __restrict__ out, int tile, int row0, int col0, int w,
                                       const u32x2 (&xv)[2], const u32x2 (&bv)[2]) {
  const int tid = threadIdx.x, lane = tid & 63;
  const int li = lane & 15, g = lane >> 4;
  const __amdgpu_buffer_rsrc_t rs = __builtin_amdgcn_make_buffer_rsrc((void*)slabs, 0, 64 * 16 * 8192, 0x00020000);
#pragma unroll
  for (int d = 0; d < 4; ++d) {
    if (d == SK) continue;
    const f32x4 p0 = r[d][0], p1 = r[d][1];
    u32x4 o;
    o[0] = pack2(p0[0], p0[1]);
    o[1] = pack2(p0[2], p0[3]);
    o[2] = pack2(p1[0], p1[1]);
    o[3] = pack2(p1[2], p1[3]);
    __builtin_amdgcn_raw_buffer_store_b128(o, rs, ((tile * 4 + d) * 4 + SK) * 8192 + w * 1024 + lane * 16, 0, 16);
  }
  const int row = row0 + SK * 16 + li;
  const int colb = col0 + 4 * g;
  WAIT_V(0);
  STAMP2(6);
  __syncthreads();
  if (tid == 0) {
    unsigned* c = cnt + tile * 64;
    __hip_atomic_fetch_add(c, 1u, __ATOMIC_RELAXED, __HIP_MEMORY_SCOPE_AGENT);
    unsigned spins = 0;
    while (__hip_atomic_load(c, __ATOMIC_RELAXED, __HIP_MEMORY_SCOPE_AGENT) < 4u) {
      __builtin_amdgcn_s_sleep(1);
      if (++spins > (1u << 24)) break;
    }
  }
  __syncthreads();
  STAMP2(7);
  u32x4 part[4];
#pragma unroll
  for (int s = 0; s < 4; ++s)
    if (s != SK) part[s] = __builtin_amdgcn_raw_buffer_load_b128(rs, ((tile * 4 + SK) * 4 + s) * 8192 + w * 1024 + lane * 16, 0, 16);
#pragma unroll
  for (int j = 0; j < 2; ++j) {
    float sum[4];
#pragma unroll
    for (int i = 0; i < 4; ++i) {
      float t = r[SK][j][i];
#pragma unroll
      for (int s = 0; s < 4; ++s)
        if (s != SK) t = aadd(t, (i & 1) ? bf_hi(part[s][2 * j + (i >> 1)]) : bf_lo(part[s][2 * j + (i >> 1)]));
      sum[i] = t;
    }
    u32x2 o;
    o[0] = pack2(aadd(aadd(bf_lo(xv[j][0]), sum[0]), bf_lo(bv[j][0])), aadd(aadd(bf_hi(xv[j][0]), sum[1]), bf_hi(bv[j][0])));
    o[1] = pack2(aadd(aadd(bf_lo(xv[j][1]), sum[2]), bf_lo(bv[j][1])), aadd(aadd(bf_hi(xv[j][1]), sum[3]), bf_hi(bv[j][1])));
    *(u32x2*)(out + row * DM + colb + j * 16) = o;
  }
  STAMP2(8);
}

__global__ __launch_bounds__(512, 2) void k2(const u16* __restrict__ Cr, const u16* __restrict__ Ci,
                                             const u16* __restrict__ H, const u16* __restrict__ BT,
                                             const u16* __restrict__ x, const u16* __restrict__ b2,
                                             u16* __restrict__ out, float* __restrict__ slabs,
                                             unsigned* __restrict__ cnt) {
  extern __shared__ __attribute__((aligned(16))) char smem[];
  const int bid = blockIdx.x;
  const int xcd = bid & 7, idx = bid >> 3;
  const int sk = xcd >> 1;
  const int mt = (xcd & 1) * 8 + (idx >> 2);
  const int nt = idx & 3;
  const int tid = threadIdx.x, lane = tid & 63;
  const int w = __builtin_amdgcn_readfirstlane(tid >> 6);
  const int ksel = w >> 2, wn = w & 3;
  const int li = lane & 15, g = lane >> 4;
  const int m0 = mt * 128, n0 = nt * 128;

  RSTAMP(1, 30);
  const int row0 = 16 * w + (lane >> 3);
  const int chE = ((lane & 7) ^ (lane >> 4)) * 8, chO = chE ^ 32;
  const int kt0 = sk * K2_NT;
  const char* bsrc = (const char*)BT + ((((n0 >> 4) + 2 * wn) * (KTOT / 32) + 2 * kt0 + ksel) * 64 + lane) * 16;

  f32x4 acc[8][2];
#pragma unroll
  for (int i = 0; i < 8; ++i)
#pragma unroll
    for (int j = 0; j < 2; ++j) acc[i][j] = (f32x4){0.f, 0.f, 0.f, 0.f};
  const int rdA = li * 128 + ((4 * ksel + g) ^ (li >> 1)) * 16;
  bf16x8 af[8];

#define K2_ISSUE(T, S)                                                                                         \
  do {                                                                                                         \
    const int kt_ = kt0 + (T);                                                                                 \
    char* dst_ = smem + (S) * K2_TILE + w * 2048;                                                              \
    const u16* ap_ = (kt_ < 32) ? (Cr + kt_ * 64) : (kt_ < 64) ? (Ci + (kt_ - 32) * 64) : (H + (kt_ - 64) * 64); \
    const int pitch_ = (kt_ < 64) ? 2048 : FF;                                                                 \
    const u16* asrc_ = ap_ + (m0 + row0) * pitch_;                                                             \
    glds16(asrc_ + chE, dst_);                                                                                 \
    glds16(asrc_ + 8 * pitch_ + chO, dst_ + 1024);                                                             \
    const char* bp_ = bsrc + (T) * 2048;                                                                       \
    asm volatile("global_load_dwordx4 a[%c2:%c3], %0, off\n\tglobal_load_dwordx4 a[%c4:%c5], %1, off"          \
                 :                                                                                             \
                 : "v"(bp_), "v"(bp_ + (KTOT / 32) * 1024), "i"(8 * (S)), "i"(8 * (S) + 3), "i"(8 * (S) + 4),  \
                   "i"(8 * (S) + 7)                                                                            \
                 : "memory", K2_ACLOB);                                                                        \
  } while (0)
#define K2_RD(S)                                                                                               \
  do {                                                                                                         \
    _Pragma("unroll") for (int i_ = 0; i_ < 8; ++i_) af[i_] = *(const bf16x8*)(smem + (S) * K2_TILE + rdA + i_ * 2048); \
  } while (0)
#define K2_MM4(S, I)                                                                                           \
  asm volatile("s_nop 1\n\tv_mfma_f32_16x16x32_bf16 %0, a[%c12:%c13], %8, %0\n\tv_mfma_f32_16x16x32_bf16 %1, a[%c14:%c15], %8, %1\n\t" \
      "v_mfma_f32_16x16x32_bf16 %2, a[%c12:%c13], %9, %2\n\tv_mfma_f32_16x16x32_bf16 %3, a[%c14:%c15], %9, %3\n\t"  \
      "v_mfma_f32_16x16x32_bf16 %4, a[%c12:%c13], %10, %4\n\tv_mfma_f32_16x16x32_bf16 %5, a[%c14:%c15], %10, %5\n\t" \
      "v_mfma_f32_16x16x32_bf16 %6, a[%c12:%c13], %11, %6\n\tv_mfma_f32_16x16x32_bf16 %7, a[%c14:%c15], %11, %7"     \
      : "+a"(acc[(I)][0]), "+a"(acc[(I)][1]), "+a"(acc[(I) + 1][0]), "+a"(acc[(I) + 1][1]), "+a"(acc[(I) + 2][0]),  \
        "+a"(acc[(I) + 2][1]), "+a"(acc[(I) + 3][0]), "+a"(acc[(I) + 3][1])                                    \
      : "v"(af[(I)]), "v"(af[(I) + 1]), "v"(af[(I) + 2]), "v"(af[(I) + 3]), "i"(8 * (S)), "i"(8 * (S) + 3),    \
        "i"(8 * (S) + 4), "i"(8 * (S) + 7))
#define K2_MM(S) do { K2_MM4(S, 0); K2_MM4(S, 4); } while (0)
#define K2_STEP_A(T, S, NV, ISS)                    \
  do {                                              \
    wait_vm<NV>();                                  \
    WAIT_L0();                                      \
    BAR();                                          \
    if (ISS) K2_ISSUE((T) + 3, ((S) + 3) & 3);      \
    K2_RD(S);                                       \
    K2_MM(S);                                       \
    __builtin_amdgcn_sched_barrier(0);              \
  } while (0)
#define K2_STEP_B(T, S, NV, ISS)                    \
  do {                                              \
    wait_vm<NV>();                                  \
    WAIT_L0();                                      \
    BAR();                                          \
    K2_MM(((S) + 3) & 3);                           \
    __builtin_amdgcn_sched_barrier(0);              \
    if (ISS) K2_ISSUE((T) + 3, ((S) + 3) & 3);      \
    K2_RD(S);                                       \
    __builtin_amdgcn_sched_barrier(0);              \
  } while (0)

  const int rw0 = m0 + ksel * 64, cl0 = n0 + wn * 32;
  u32x2 xv[2], bv[2];
#pragma unroll
  for (int j = 0; j < 2; ++j) {
    xv[j] = *(const u32x2*)(x + (rw0 + sk * 16 + li) * DM + cl0 + 4 * g + j * 16);
    bv[j] = *(const u32x2*)(b2 + cl0 + 4 * g + j * 16);
  }
#pragma unroll
  for (int i = 0; i < 8; ++i) asm volatile("s_nop 1" : "+a"(acc[i][0]), "+a"(acc[i][1]));
  STAMP2(0);
  K2_ISSUE(0, 0);
  K2_ISSUE(1, 1);
  K2_ISSUE(2, 2);
  if (w < 4) {
#pragma unroll 1
    for (int t = 0; t < 20; t += 4) {
      K2_STEP_A(t, 0, 8, true);
      K2_STEP_A(t + 1, 1, 8, true);
      K2_STEP_A(t + 2, 2, 8, true);
      K2_STEP_A(t + 3, 3, 8, true);
    }
    K2_STEP_A(20, 0, 8, false);
    K2_STEP_A(21, 1, 4, false);
    K2_STEP_A(22, 2, 0, false);
  } else {
    wait_vm<8>();
    BAR();
    K2_ISSUE(3, 3);
    K2_RD(0);
    K2_STEP_B(1, 1, 8, true);
    K2_STEP_B(2, 2, 8, true);
    K2_STEP_B(3, 3, 8, true);
#pragma unroll 1
    for (int t = 4; t < 20; t += 4) {
      K2_STEP_B(t, 0, 8, true);
      K2_STEP_B(t + 1, 1, 8, true);
      K2_STEP_B(t + 2, 2, 8, true);
      K2_STEP_B(t + 3, 3, 8, true);
    }
    K2_STEP_B(20, 0, 8, false);
    K2_STEP_B(21, 1, 4, false);
    K2_STEP_B(22, 2, 0, false);
    K2_MM(2);
  }
  STAMP2(5);
  f32x4 r[4][2];
#pragma unroll
  for (int i = 0; i < 8; ++i) asm volatile("s_nop 7" : "+a"(acc[i][0]), "+a"(acc[i][1]));
  WAIT_L0();
  BAR();
  {
    char* mine = smem + w * 8192 + lane * 16;
    const char* theirs = smem + (w ^ 4) * 8192 + lane * 16;
    if (ksel == 0) {
#pragma unroll
      for (int i = 0; i < 4; ++i)
#pragma unroll
        for (int j = 0; j < 2; ++j) { *(f32x4*)(mine + (i * 2 + j) * 1024) = acc[4 + i][j]; r[i][j] = acc[i][j]; }
    } else {
#pragma unroll
      for (int i = 0; i < 4; ++i)
#pragma unroll
        for (int j = 0; j < 2; ++j) { *(f32x4*)(mine + (i * 2 + j) * 1024) = acc[i][j]; r[i][j] = acc[4 + i][j]; }
    }
    WAIT_L0();
    BAR();
    f32x4 o[4][2];
#pragma unroll
    for (int i = 0; i < 4; ++i)
#pragma unroll
      for (int j = 0; j < 2; ++j) o[i][j] = *(const f32x4*)(theirs + (i * 2 + j) * 1024);
#pragma unroll
    for (int i = 0; i < 4; ++i)
#pragma unroll
      for (int j = 0; j < 2; ++j)
#pragma unroll
        for (int q = 0; q < 4; ++q) r[i][j][q] = aadd(r[i][j][q], o[i][j][q]);
  }
  const int tile = mt * 4 + nt;
  if (sk == 0) k2_epi<0>(r, slabs, cnt, x, b2, out, tile, rw0, cl0, w, xv, bv);
  else if (sk == 1) k2_epi<1>(r, slabs, cnt, x, b2, out, tile, rw0, cl0, w, xv, bv);
  else if (sk == 2) k2_epi<2>(r, slabs, cnt, x, b2, out, tile, rw0, cl0, w, xv, bv);
  else k2_epi<3>(r, slabs, cnt, x, b2, out, tile, rw0, cl0, w, xv, bv);
  RSTAMP(1, 31);
}

extern "C" void kernel_launch(void* const* d_in, const int* in_sizes, int n_in, void* d_out, int out_size,
                              void* d_ws, size_t ws_size, hipStream_t stream) {
  const u16* x = (const u16*)d_in[0];
  const u16* Cr = (const u16*)d_in[1];
  const u16* Ci = (const u16*)d_in[2];
  const u16* Wr = (const u16*)d_in[3];
  const u16* Wi = (const u16*)d_in[4];
  const u16* w1 = (const u16*)d_in[6];
  const u16* b1 = (const u16*)d_in[7];
  const u16* w2 = (const u16*)d_in[8];
  const u16* b2 = (const u16*)d_in[9];
  u16* out = (u16*)d_out;
  char* ws = (char*)d_ws;
  u16* BT = (u16*)ws;
  u16* H = (u16*)(ws + 6029312);
  float* slabs = (float*)(ws + 6029312 + 7340032);
  unsigned* cnt = (unsigned*)(ws + 6029312 + 7340032 + 16777216);

  (void)hipFuncSetAttribute((const void*)k1, hipFuncAttributeMaxDynamicSharedMemorySize, K1_LDS);
  (void)hipFuncSetAttribute((const void*)k2, hipFuncAttributeMaxDynamicSharedMemorySize, K2_LDS);
  k1<<<256, 512, K1_LDS, stream>>>(x, Wr, Wi, w1, b1, w2, BT, H, cnt, Cr, Ci);
  k2<<<256, 512, K2_LDS, stream>>>(Cr, Ci, H, BT, x, b2, out, slabs, cnt);
}
```

```cpp
#include <hip/hip_runtime.h>
#include <hip/hip_bf16.h>
#include <stdint.h>

typedef unsigned short u16;
typedef __attribute__((ext_vector_type(8))) short bf16x8;
typedef __attribute__((ext_vector_type(4))) short bf16x4;
typedef __attribute__((ext_vector_type(2))) short bf16x2;
typedef __attribute__((ext_vector_type(4))) float f32x4;
typedef __attribute__((ext_vector_type(4))) unsigned int u32x4;
typedef __attribute__((ext_vector_type(2))) unsigned int u32x2;

#define SEQ 2048
#define DM 512
#define FF 1792
#define KTOT 5888

#define STAMP(i)
#define STAMP2(i)
#define RSTAMP(k, i)
#define WAIT_V(n) asm volatile("s_waitcnt vmcnt(" #n ")" ::: "memory")
#define WAIT_L0() asm volatile("s_waitcnt lgkmcnt(0)" ::: "memory")
#define BAR() asm volatile("s_barrier" ::: "memory")

__device__ __forceinline__ float bf2f(u16 v) { return __uint_as_float(((unsigned)v) << 16); }
__device__ __forceinline__ float bf_lo(unsigned v) { return __uint_as_float(v << 16); }
__device__ __forceinline__ float bf_hi(unsigned v) { return __uint_as_float(v & 0xffff0000u); }
__device__ __forceinline__ unsigned pack2(float lo, float hi) {
  unsigned r;
  asm("v_cvt_pk_bf16_f32 %0, %1, %2" : "=v"(r) : "v"(lo), "v"(hi));
  return r;
}
__device__ __forceinline__ float rbf(float f) { return bf_lo(pack2(f, f)); }
__device__ __forceinline__ float afma(float a, float b, float c) {
  float r;
  asm("v_fma_f32 %0, %1, %2, %3" : "=v"(r) : "v"(a), "v"(b), "v"(c));
  return r;
}
__device__ __forceinline__ float aadd(float a, float b) {
  float r;
  asm("v_add_f32 %0, %1, %2" : "=v"(r) : "v"(a), "v"(b));
  return r;
}
__device__ __forceinline__ float amul(float a, float b) {
  float r;
  asm("v_mul_f32 %0, %1, %2" : "=v"(r) : "v"(a), "v"(b));
  return r;
}

template <int N> __device__ __forceinline__ void wait_vm() {
  asm volatile("s_waitcnt vmcnt(%0)" ::"i"(N) : "memory");
}

__device__ __forceinline__ void glds16(const void* g, void* l) {
  __builtin_amdgcn_global_load_lds((const __attribute__((address_space(1))) unsigned*)g,
                                   (__attribute__((address_space(3))) unsigned*)l, 16, 0, 0);
}

__device__ constexpr int k1_allowed(int kc, bool pf) {
  return (kc + 2 <= 7 ? 5 : 0) + (pf ? ((kc - 2 >= 1 ? 1 : 0) + (kc - 1 >= 1 ? 1 : 0)) : 0);
}
#define K1_CHUNK 38912
#define K1_NBUF 4
#define K1_STATS (K1_NBUF * K1_CHUNK)
#define K1_BIAS (K1_STATS + 1024)
#define K1_LDS (K1_BIAS + 512)

template <int NAB, int WN>
__device__ __forceinline__ void k1_body(const u16* __restrict__ x, const u16* __restrict__ Wr,
                                        const u16* __restrict__ Wi, const u16* __restrict__ w1,
                                        const u16* __restrict__ b1, u16* __restrict__ BT,
                                        u16* __restrict__ H, char* smem, int mt, int nt,
                                        const u16* Cr, const u16* Ci, const u32x4 w2v, int bid) {
  constexpr int NH = 11 - NAB;
  constexpr int ROT = (NH == 11) ? 4 : 1;
  constexpr int NHD = (NH == 0) ? 1 : NH;
  constexpr int NB0 = 6;
  constexpr int wn = WN;
  constexpr int NBW = (wn == 0) ? 6 : 5;
  constexpr bool PF = NAB > 0;
  const int tid = threadIdx.x;
  const int lane = tid & 63;
  const int w = __builtin_amdgcn_readfirstlane(tid >> 6);
  const int wm = w & 3;
  const int li = lane & 15, g = lane >> 4;
  const int c0 = nt * 176;
  const int m0 = mt * 128 + wm * 32;

  STAMP(0);
  const u16* src[5];
  int inc[5], jd[5];
#pragma unroll
  for (int jj = 0; jj < 2; ++jj) {
    const int row = 16 * w + 8 * jj + (lane >> 3);
    const int sw = (((row >> 3) & 3) << 1) | ((row >> 1) & 1);
    src[jj] = x + (mt * 128 + row) * DM + ((lane & 7) ^ sw) * 8;
    inc[jj] = 64;
    jd[jj] = (2 * w + jj) * 1024;
  }
#pragma unroll
  for (int jj = 0; jj < 3; ++jj) {
    int j = w + 8 * jj;
    if (j > 21) j -= 8;
    jd[2 + jj] = 16384 + j * 1024;
    if (NAB == 11 || (NAB > 0 && j < 2 * NAB)) {
      const int row = 8 * j + (lane >> 3);
      const int ch = (lane & 7) ^ ((row >> 1) & 7);
      const int n = c0 + row;
      const u16* base = (n < 512) ? (Wr + n * DM) : (Wi + (n - 512) * DM);
      src[2 + jj] = base + ch * 8;
      inc[2 + jj] = 64;
    } else {
      const unsigned p16 = (j - 2 * NAB) * 64 + lane;
      const unsigned r = p16 / (2 * NHD);
      const unsigned rem = p16 - r * (2 * NHD);
      const int cp = rem >> 1, hf = rem & 1;
      int c = cp - ROT * ((r >> 3) & 1);
      if (c < 0) c += NH;
      src[2 + jj] = w1 + r * FF + (c0 + 16 * NAB - 1024) + 16 * c + 8 * hf;
      inc[2 + jj] = 64 * FF;
    }
  }
  auto issue = [&](int kc) {
#pragma unroll
    for (int jj = 0; jj < 5; ++jj) glds16(src[jj] + kc * inc[jj], smem + (kc % K1_NBUF) * K1_CHUNK + jd[jj]);
  };
  u32x2 bias_v = {0u, 0u};
  if (NH > 0 && tid < NH * 4) bias_v = *(const u32x2*)(b1 + (c0 + 16 * NAB - 1024) + 4 * tid);
  issue(0);
  issue(1);
  issue(2);

  f32x4 acc[2][NBW], cs[NBW];
#pragma unroll
  for (int b = 0; b < NBW; ++b) {
    acc[0][b] = (f32x4){0.f, 0.f, 0.f, 0.f};
    acc[1][b] = (f32x4){0.f, 0.f, 0.f, 0.f};
    cs[b] = (f32x4){0.f, 0.f, 0.f, 0.f};
  }
  f32x4 d1 = {0.f, 0.f, 0.f, 0.f}, d2 = {0.f, 0.f, 0.f, 0.f};
  bf16x8 ones = {0x3f80, 0x3f80, 0x3f80, 0x3f80, 0x3f80, 0x3f80, 0x3f80, 0x3f80};
  asm volatile("" : "+v"(ones));

  const int q4 = li >> 2, p4 = lane & 3;
  const int laneX0 = (wm * 32 + 8 * q4 + (li & 3)) * 128 + ((0 + g) ^ (li >> 1)) * 16;
  const int laneX1 = (wm * 32 + 8 * q4 + (li & 3)) * 128 + ((4 + g) ^ (li >> 1)) * 16;
  const int laneAB0 = 16384 + li * 128 + ((0 + g) ^ (li >> 1)) * 16;
  const int laneAB1 = 16384 + li * 128 + ((4 + g) ^ (li >> 1)) * 16;
  const int laneH0 = 16384 + NAB * 2048 + (8 * g + q4) * (NHD * 32) + (g & 1) * ROT * 32 + p4 * 8;
  const int laneH1 = laneH0 - (g & 1) * NH * 32;
  int boff = 0;
  unsigned pfv[6] = {0u, 0u, 0u, 0u, 0u, 0u};
  auto setbuf = [&](int kc) {
    boff = (kc % K1_NBUF) * K1_CHUNK;
    asm volatile("" : "+s"(boff));
  };
  bf16x8 wf[NBW], xa[2][2];
  auto rdx = [&](int step) {
    const int ks = step & 1;
    xa[ks][0] = *(const bf16x8*)(smem + boff + (ks ? laneX1 : laneX0));
    xa[ks][1] = *(const bf16x8*)(smem + boff + (ks ? laneX1 : laneX0) + 512);
  };
  auto rdw = [&](int step, int bb) {
    const int ks = step & 1;
    const int b = wn * NB0 + bb;
    if (b < NAB) {
      wf[bb] = *(const bf16x8*)(smem + boff + (ks ? laneAB1 : laneAB0) + b * 2048);
    } else {
      const int hb = b - NAB;
      const char* hp = smem + boff + ((hb + ROT >= NH) ? laneH1 : laneH0) + 32 * ks * (NHD * 32) + hb * 32;
      const bf16x4 t0 = __builtin_amdgcn_ds_read_tr16_b64_v4i16((__attribute__((address_space(3))) bf16x4*)(hp));
      const bf16x4 t1 = __builtin_amdgcn_ds_read_tr16_b64_v4i16((__attribute__((address_space(3))) bf16x4*)(hp + 4 * (NHD * 32)));
      wf[bb] = __builtin_shufflevector(t0, t1, 0, 1, 2, 3, 4, 5, 6, 7);
    }
  };
#pragma unroll
  for (int bb = 0; bb < NBW; ++bb) asm volatile("s_nop 1" : "+v"(acc[0][bb]), "+v"(acc[1][bb]), "+v"(cs[bb]));
  asm volatile("s_nop 1" : "+v"(d1), "+v"(d2));
  wait_vm<10>();
  BAR();
  STAMP(1);
  setbuf(0);
  rdx(0);
#pragma unroll
  for (int bb = 0; bb < NBW; ++bb) rdw(0, bb);
#pragma unroll
  for (int step = 0; step < 16; ++step) {
    const int kc = step >> 1, ks = step & 1;
    if (ks && kc < 7) {
      if (kc == 0) wait_vm<k1_allowed(0, PF)>(); else if (kc == 1) wait_vm<k1_allowed(1, PF)>(); else if (kc == 2) wait_vm<k1_allowed(2, PF)>();
      else if (kc == 3) wait_vm<k1_allowed(3, PF)>(); else if (kc == 4) wait_vm<k1_allowed(4, PF)>(); else if (kc == 5) wait_vm<k1_allowed(5, PF)>();
      else wait_vm<k1_allowed(6, PF)>();
      BAR();
      setbuf(kc + 1);
      STAMP(4 + kc);
    }
    if (step < 15) rdx(step + 1);
    asm("v_mfma_f32_16x16x32_bf16 %0, %2, %3, %0\n\tv_mfma_f32_16x16x32_bf16 %1, %2, %2, %1"
        : "+v"(d1), "+v"(d2)
        : "v"(xa[ks][wn]), "v"(ones));
    __builtin_amdgcn_sched_barrier(0);
#pragma unroll
    for (int bb = 0; bb < NBW; ++bb) {
      const int b = wn * NB0 + bb;
      if (b < NAB) {
        asm("v_mfma_f32_16x16x32_bf16 %0, %3, %5, %0\n\tv_mfma_f32_16x16x32_bf16 %1, %4, %5, %1\n\t"
            "v_mfma_f32_16x16x32_bf16 %2, %6, %5, %2"
            : "+v"(acc[0][bb]), "+v"(acc[1][bb]), "+v"(cs[bb])
            : "v"(xa[ks][0]), "v"(xa[ks][1]), "v"(wf[bb]), "v"(ones));
      } else {
        asm("v_mfma_f32_16x16x32_bf16 %0, %5, %3, %0\n\tv_mfma_f32_16x16x32_bf16 %1, %5, %4, %1\n\t"
            "v_mfma_f32_16x16x32_bf16 %2, %5, %6, %2"
            : "+v"(acc[0][bb]), "+v"(acc[1][bb]), "+v"(cs[bb])
            : "v"(xa[ks][0]), "v"(xa[ks][1]), "v"(wf[bb]), "v"(ones));
      }
      if (step < 15) rdw(step + 1, bb);
      if (ks && kc < 7 && bb < 5) {
        if (kc + 3 < 8) glds16(src[bb] + (kc + 3) * inc[bb], smem + ((kc + 3) % K1_NBUF) * K1_CHUNK + jd[bb]);
        if (bb == 4) {
          asm volatile("" ::: "memory");
          if (PF && kc >= 1) {
            unsigned sec = ((kc - 1) * 96 + mt * 6 + nt) * 512 + tid;
            sec = sec < 262144u ? sec : 262143u;
            pfv[kc - 1] = *(const unsigned*)((sec < 131072u ? (const char*)Cr : (const char*)Ci) + (size_t)(sec & 131071u) * 64);
          }
          asm volatile("" ::: "memory");
        }
      }
      __builtin_amdgcn_sched_barrier(0);
    }
  }
  STAMP(12);

  {
    asm volatile("s_nop 7" : "+v"(d1), "+v"(d2));
    const bool valid = q4 == g;
    const int rr = li & 3;
    const float s = rr == 0 ? d1[0] : rr == 1 ? d1[1] : rr == 2 ? d1[2] : d1[3];
    const float q = rr == 0 ? d2[0] : rr == 1 ? d2[1] : rr == 2 ? d2[2] : d2[3];
    float mean = amul(s, 1.0f / 512.0f);
    float varf = afma(q, 1.0f / 512.0f, -mean * mean);
    const int srow = 8 * q4 + 4 * wn + rr;
    if (__any(valid && amul(mean, mean) > amul(8.0f, varf))) {
      mean = __shfl(mean, q4 * 16 + li);
      float v0 = 0.f, v1 = 0.f;
      for (int ks = 0; ks < 16; ++ks) {
        const u32x4 v = *(const u32x4*)(x + (m0 + srow) * DM + ks * 32 + g * 8);
#pragma unroll
        for (int qq = 0; qq < 4; ++qq) {
          const float e0 = aadd(bf_lo(v[qq]), -mean), e1 = aadd(bf_hi(v[qq]), -mean);
          v0 = afma(e0, e0, v0);
          v1 = afma(e1, e1, v1);
        }
      }
      float vs = v0 + v1;
      vs += __shfl_xor(vs, 16);
      vs += __shfl_xor(vs, 32);
      varf = vs * (1.0f / 512.0f);
    }
    const float var = rbf(varf);
    const float sd = rbf(sqrtf(rbf(var + 1e-5f)));
    const float rinv = 1.0f / sd;
    const float mb16 = rbf(mean);
    if (valid) ((float2*)(smem + K1_STATS))[wm * 32 + srow] = make_float2(rinv, -mb16 * rinv);
    if (NH > 0 && tid < NH * 4) *(u32x2*)(smem + K1_BIAS + tid * 8) = bias_v;
  }
  WAIT_L0();
  BAR();
  if (bid < 224) {
    u16* T = (u16*)(smem + 65536);
    const int f = tid >> 3, e8 = (tid & 7) * 8;
#pragma unroll
    for (int q = 0; q < 4; ++q) {
      T[(e8 + 2 * q) * 72 + f] = (u16)(w2v[q] & 0xffffu);
      T[(e8 + 2 * q + 1) * 72 + f] = (u16)(w2v[q] >> 16);
    }
  }

  const __amdgpu_buffer_rsrc_t rsB = __builtin_amdgcn_make_buffer_rsrc((void*)BT, 0, DM * KTOT * 2, 0x00020000);
  const float2* stp = (const float2*)(smem + K1_STATS) + wm * 32;
  float2 sta[8];
  float2 sth[2];
  if (NAB > wn * NB0) {
#pragma unroll
    for (int i = 0; i < 8; ++i) sta[i] = stp[8 * g + i];
  }
  if (NAB < 11 && !(wn == 0 && NAB >= NB0)) {
#pragma unroll
    for (int mb = 0; mb < 2; ++mb) sth[mb] = stp[8 * q4 + 4 * mb + (li & 3)];
  }
#pragma unroll
  for (int bb = 0; bb < NBW; ++bb) {
    const int b = wn * NB0 + bb;
    if (b < NAB) {
      const int n = c0 + 16 * b + li;
      const bool isb = n >= 512;
      const unsigned sgn = isb ? 0x80008000u : 0u;
      u32x4 o;
#pragma unroll
      for (int mb = 0; mb < 2; ++mb) {
        float t0, t1, t2, t3;
        unsigned o0, o1;
        asm("v_mul_f32 %2, %11, %10\n\tv_mul_f32 %3, %13, %10\n\tv_mul_f32 %4, %15, %10\n\tv_mul_f32 %5, %17, %10\n\t"
            "v_fma_f32 %2, %12, %6, %2\n\tv_fma_f32 %3, %14, %7, %3\n\tv_fma_f32 %4, %16, %8, %4\n\tv_fma_f32 %5, %18, %9, %5\n\t"
            "v_cvt_pk_bf16_f32 %0, %2, %3\n\tv_cvt_pk_bf16_f32 %1, %4, %5\n\t"
            "v_xor_b32 %0, %19, %0\n\tv_xor_b32 %1, %19, %1"
            : "=&v"(o0), "=&v"(o1), "=&v"(t0), "=&v"(t1), "=&v"(t2), "=&v"(t3)
            : "v"(acc[mb][bb][0]), "v"(acc[mb][bb][1]), "v"(acc[mb][bb][2]), "v"(acc[mb][bb][3]), "v"(cs[bb][0]),
              "v"(sta[4 * mb + 0].y), "v"(sta[4 * mb + 0].x), "v"(sta[4 * mb + 1].y), "v"(sta[4 * mb + 1].x),
              "v"(sta[4 * mb + 2].y), "v"(sta[4 * mb + 2].x), "v"(sta[4 * mb + 3].y), "v"(sta[4 * mb + 3].x),
              "v"(sgn));
        o[2 * mb] = o0;
        o[2 * mb + 1] = o1;
      }
      __builtin_amdgcn_raw_buffer_store_b128(o, rsB, ((((n & 511) >> 4) * (KTOT / 32) + (((isb ? 2048 : 0) + m0) >> 5)) * 64 + lane) * 16, 0, 16);
    } else {
      const int fl = 16 * (b - NAB) + 4 * g;
      const u32x2 bv = *(const u32x2*)(smem + K1_BIAS + fl * 2);
      const float bias0 = bf_lo(bv[0]), bias1 = bf_hi(bv[0]), bias2 = bf_lo(bv[1]), bias3 = bf_hi(bv[1]);
#pragma unroll
      for (int mb = 0; mb < 2; ++mb) {
        float t0, t1, t2, t3;
        unsigned o0, o1;
        asm("v_fma_f32 %2, %19, %10, %14\n\tv_fma_f32 %3, %19, %11, %15\n\tv_fma_f32 %4, %19, %12, %16\n\tv_fma_f32 %5, %19, %13, %17\n\t"
            "v_fma_f32 %2, %18, %6, %2\n\tv_fma_f32 %3, %18, %7, %3\n\tv_fma_f32 %4, %18, %8, %4\n\tv_fma_f32 %5, %18, %9, %5\n\t"
            "v_max_f32 %2, 0, %2\n\tv_max_f32 %3, 0, %3\n\tv_max_f32 %4, 0, %4\n\tv_max_f32 %5, 0, %5\n\t"
            "v_mul_f32 %2, %2, %2\n\tv_mul_f32 %3, %3, %3\n\tv_mul_f32 %4, %4, %4\n\tv_mul_f32 %5, %5, %5\n\t"
            "v_cvt_pk_bf16_f32 %0, %2, %3\n\tv_cvt_pk_bf16_f32 %1, %4, %5"
            : "=&v"(o0), "=&v"(o1), "=&v"(t0), "=&v"(t1), "=&v"(t2), "=&v"(t3)
            : "v"(acc[mb][bb][0]), "v"(acc[mb][bb][1]), "v"(acc[mb][bb][2]), "v"(acc[mb][bb][3]),
              "v"(cs[bb][0]), "v"(cs[bb][1]), "v"(cs[bb][2]), "v"(cs[bb][3]),
              "v"(bias0), "v"(bias1), "v"(bias2), "v"(bias3), "v"(sth[mb].x), "v"(sth[mb].y));
        u32x2 o;
        o[0] = o0;
        o[1] = o1;
        const int row = wm * 32 + 8 * q4 + 4 * mb + (li & 3);
        *(u32x2*)(smem + row * (NHD * 32) + fl * 2) = o;
      }
    }
  }
  WAIT_L0();
  BAR();
  if (bid < 224) {
    const int e = tid >> 3, f8 = (tid & 7) * 8;
    const u32x4 v = *(const u32x4*)(smem + 65536 + (e * 72 + f8) * 2);
    const int n = (bid & 7) * 64 + e, k = 4096 + (bid >> 3) * 64 + f8;
    __builtin_amdgcn_raw_buffer_store_b128(v, rsB, (((n >> 4) * (KTOT / 32) + (k >> 5)) * 64 + ((k >> 3) & 3) * 16 + (n & 15)) * 16, 0, 16);
  }
  if (NH > 0) {
    const __amdgpu_buffer_rsrc_t rsH = __builtin_amdgcn_make_buffer_rsrc((void*)H, 0, SEQ * FF * 2, 0x00020000);
    const int hoff = ((mt * 128) * FF + (c0 + 16 * NAB - 1024)) * 2;
#pragma unroll
    for (int i = 0; i < (128 * NHD * 2 + 511) / 512; ++i) {
      const unsigned id = tid + 512 * i;
      if (128 * NHD * 2 % 512 == 0 || id < 128 * NHD * 2) {
        const unsigned row = id / (NHD * 2), c = id - row * (NHD * 2);
        __builtin_amdgcn_raw_buffer_store_b128(*(const u32x4*)(smem + id * 16), rsH, hoff + (row * FF + c * 8) * 2, 0, 16);
      }
    }
  }
  if (PF) asm volatile("" ::"v"(pfv[0]), "v"(pfv[1]), "v"(pfv[2]), "v"(pfv[3]), "v"(pfv[4]), "v"(pfv[5]));
}

__global__ __launch_bounds__(512, 2) void k1(const u16* __restrict__ x, const u16* __restrict__ Wr,
                                             const u16* __restrict__ Wi, const u16* __restrict__ w1,
                                             const u16* __restrict__ b1, const u16* __restrict__ w2,
                                             u16* __restrict__ BT, u16* __restrict__ H,
                                             unsigned* __restrict__ cnt, const u16* __restrict__ Cr,
                                             const u16* __restrict__ Ci) {
  extern __shared__ __attribute__((aligned(16))) char smem[];
  const int bid = blockIdx.x;
  const int xcd = bid & 7, idx = bid >> 3;
  const int mt = (xcd & 1) * 8 + (idx >> 2);
  const int nt = (xcd >> 1) * 4 + (idx & 3);
  const int tid = threadIdx.x;

  RSTAMP(0, 30);
  const int tf0 = (bid >> 3) * 64, te0 = (bid & 7) * 64;
  u32x4 w2v = {0u, 0u, 0u, 0u};
  if (bid < 224) w2v = *(const u32x4*)(w2 + (tf0 + (tid >> 3)) * DM + te0 + (tid & 7) * 8);
  if (bid == 0 && tid < 64) cnt[tid * 64] = 0u;

  const int wn = __builtin_amdgcn_readfirstlane(tid >> 8);
  if (wn == 0) {
    if (nt <= 4) k1_body<11, 0>(x, Wr, Wi, w1, b1, BT, H, smem, mt, nt, Cr, Ci, w2v, bid);
    else if (nt == 5) k1_body<9, 0>(x, Wr, Wi, w1, b1, BT, H, smem, mt, nt, Cr, Ci, w2v, bid);
    else k1_body<0, 0>(x, Wr, Wi, w1, b1, BT, H, smem, mt, nt, Cr, Ci, w2v, bid);
  } else {
    if (nt <= 4) k1_body<11, 1>(x, Wr, Wi, w1, b1, BT, H, smem, mt, nt, Cr, Ci, w2v, bid);
    else if (nt == 5) k1_body<9, 1>(x, Wr, Wi, w1, b1, BT, H, smem, mt, nt, Cr, Ci, w2v, bid);
    else k1_body<0, 1>(x, Wr, Wi, w1, b1, BT, H, smem, mt, nt, Cr, Ci, w2v, bid);
  }
  STAMP(13);
  RSTAMP(0, 31);
}

#define K2_NBUF 4
#define K2_TILE 16384
#define K2_LDS 65536
#define K2_NT 23
#define K2_ACLOB "a0", "a1", "a2", "a3", "a4", "a5", "a6", "a7", "a8", "a9", "a10", "a11", "a12", "a13", "a14", "a15", \
                 "a16", "a17", "a18", "a19", "a20", "a21", "a22", "a23", "a24", "a25", "a26", "a27", "a28", "a29", "a30", "a31"

template <int SK>
__device__ __forceinline__ void k2_epi(f32x4 (&r)[4][2], float* __restrict__ slabs, unsigned* __restrict__ cnt,
                                       const u16* __restrict__ x, const u16* __restrict__ b2,
                                       u16* __restrict__ out, int tile, int row0, int col0, int w,
                                       const u32x2 (&xv)[2], const u32x2 (&bv)[2]) {
  const int tid = threadIdx.x, lane = tid & 63;
  const int li = lane & 15, g = lane >> 4;
  const __amdgpu_buffer_rsrc_t rs = __builtin_amdgcn_make_buffer_rsrc((void*)slabs, 0, 64 * 16 * 8192, 0x00020000);
#pragma unroll
  for (int d = 0; d < 4; ++d) {
    if (d == SK) continue;
    const f32x4 p0 = r[d][0], p1 = r[d][1];
    u32x4 o;
    o[0] = pack2(p0[0], p0[1]);
    o[1] = pack2(p0[2], p0[3]);
    o[2] = pack2(p1[0], p1[1]);
    o[3] = pack2(p1[2], p1[3]);
    __builtin_amdgcn_raw_buffer_store_b128(o, rs, ((tile * 4 + d) * 4 + SK) * 8192 + w * 1024 + lane * 16, 0, 16);
  }
  const int row = row0 + SK * 16 + li;
  const int colb = col0 + 4 * g;
  WAIT_V(0);
  STAMP2(6);
  __syncthreads();
  if (tid == 0) {
    unsigned* c = cnt + tile * 64;
    __hip_atomic_fetch_add(c, 1u, __ATOMIC_RELAXED, __HIP_MEMORY_SCOPE_AGENT);
    unsigned spins = 0;
    while (__hip_atomic_load(c, __ATOMIC_RELAXED, __HIP_MEMORY_SCOPE_AGENT) < 4u) {
      if (++spins > (1u << 22)) break;
    }
  }
  __syncthreads();
  STAMP2(7);
  u32x4 part[4];
#pragma unroll
  for (int s = 0; s < 4; ++s)
    if (s != SK) part[s] = __builtin_amdgcn_raw_buffer_load_b128(rs, ((tile * 4 + SK) * 4 + s) * 8192 + w * 1024 + lane * 16, 0, 16);
#pragma unroll
  for (int j = 0; j < 2; ++j) {
    float sum[4];
#pragma unroll
    for (int i = 0; i < 4; ++i) {
      float t = r[SK][j][i];
#pragma unroll
      for (int s = 0; s < 4; ++s)
        if (s != SK) t = aadd(t, (i & 1) ? bf_hi(part[s][2 * j + (i >> 1)]) : bf_lo(part[s][2 * j + (i >> 1)]));
      sum[i] = t;
    }
    u32x2 o;
    o[0] = pack2(aadd(aadd(bf_lo(xv[j][0]), sum[0]), bf_lo(bv[j][0])), aadd(aadd(bf_hi(xv[j][0]), sum[1]), bf_hi(bv[j][0])));
    o[1] = pack2(aadd(aadd(bf_lo(xv[j][1]), sum[2]), bf_lo(bv[j][1])), aadd(aadd(bf_hi(xv[j][1]), sum[3]), bf_hi(bv[j][1])));
    *(u32x2*)(out + row * DM + colb + j * 16) = o;
  }
  STAMP2(8);
}

__global__ __launch_bounds__(512, 2) void k2(const u16* __restrict__ Cr, const u16* __restrict__ Ci,
                                             const u16* __restrict__ H, const u16* __restrict__ BT,
                                             const u16* __restrict__ x, const u16* __restrict__ b2,
                                             u16* __restrict__ out, float* __restrict__ slabs,
                                             unsigned* __restrict__ cnt) {
  extern __shared__ __attribute__((aligned(16))) char smem[];
  const int bid = blockIdx.x;
  const int xcd = bid & 7, idx = bid >> 3;
  const int sk = xcd >> 1;
  const int mt = (xcd & 1) * 8 + (idx >> 2);
  const int nt = idx & 3;
  const int tid = threadIdx.x, lane = tid & 63;
  const int w = __builtin_amdgcn_readfirstlane(tid >> 6);
  const int ksel = w >> 2, wn = w & 3;
  const int li = lane & 15, g = lane >> 4;
  const int m0 = mt * 128, n0 = nt * 128;

  RSTAMP(1, 30);
  const int row0 = 16 * w + (lane >> 3);
  const int chE = ((lane & 7) ^ (lane >> 4)) * 8, chO = chE ^ 32;
  const int kt0 = sk * K2_NT;
  const char* bsrc = (const char*)BT + ((((n0 >> 4) + 2 * wn) * (KTOT / 32) + 2 * kt0 + ksel) * 64 + lane) * 16;

  f32x4 acc[8][2];
#pragma unroll
  for (int i = 0; i < 8; ++i)
#pragma unroll
    for (int j = 0; j < 2; ++j) acc[i][j] = (f32x4){0.f, 0.f, 0.f, 0.f};
  const int rdA = li * 128 + ((4 * ksel + g) ^ (li >> 1)) * 16;
  bf16x8 af[8];

#define K2_ISSUE(T, S)                                                                                         \
  do {                                                                                                         \
    const int kt_ = kt0 + (T);                                                                                 \
    char* dst_ = smem + (S) * K2_TILE + w * 2048;                                                              \
    const u16* ap_ = (kt_ < 32) ? (Cr + kt_ * 64) : (kt_ < 64) ? (Ci + (kt_ - 32) * 64) : (H + (kt_ - 64) * 64); \
    const int pitch_ = (kt_ < 64) ? 2048 : FF;                                                                 \
    const u16* asrc_ = ap_ + (m0 + row0) * pitch_;                                                             \
    glds16(asrc_ + chE, dst_);                                                                                 \
    glds16(asrc_ + 8 * pitch_ + chO, dst_ + 1024);                                                             \
    const char* bp_ = bsrc + (T) * 2048;                                                                       \
    asm volatile("global_load_dwordx4 a[%c2:%c3], %0, off\n\tglobal_load_dwordx4 a[%c4:%c5], %1, off"          \
                 :                                                                                             \
                 : "v"(bp_), "v"(bp_ + (KTOT / 32) * 1024), "i"(8 * (S)), "i"(8 * (S) + 3), "i"(8 * (S) + 4),  \
                   "i"(8 * (S) + 7)                                                                            \
                 : "memory", K2_ACLOB);                                                                        \
  } while (0)
#define K2_RD(S)                                                                                               \
  do {                                                                                                         \
    _Pragma("unroll") for (int i_ = 0; i_ < 8; ++i_) af[i_] = *(const bf16x8*)(smem + (S) * K2_TILE + rdA + i_ * 2048); \
  } while (0)
#define K2_MM4(S, I)                                                                                           \
  asm volatile("s_nop 1\n\tv_mfma_f32_16x16x32_bf16 %0, a[%c12:%c13], %8, %0\n\tv_mfma_f32_16x16x32_bf16 %1, a[%c14:%c15], %8, %1\n\t" \
      "v_mfma_f32_16x16x32_bf16 %2, a[%c12:%c13], %9, %2\n\tv_mfma_f32_16x16x32_bf16 %3, a[%c14:%c15], %9, %3\n\t"  \
      "v_mfma_f32_16x16x32_bf16 %4, a[%c12:%c13], %10, %4\n\tv_mfma_f32_16x16x32_bf16 %5, a[%c14:%c15], %10, %5\n\t" \
      "v_mfma_f32_16x16x32_bf16 %6, a[%c12:%c13], %11, %6\n\tv_mfma_f32_16x16x32_bf16 %7, a[%c14:%c15], %11, %7"     \
      : "+a"(acc[(I)][0]), "+a"(acc[(I)][1]), "+a"(acc[(I) + 1][0]), "+a"(acc[(I) + 1][1]), "+a"(acc[(I) + 2][0]),  \
        "+a"(acc[(I) + 2][1]), "+a"(acc[(I) + 3][0]), "+a"(acc[(I) + 3][1])                                    \
      : "v"(af[(I)]), "v"(af[(I) + 1]), "v"(af[(I) + 2]), "v"(af[(I) + 3]), "i"(8 * (S)), "i"(8 * (S) + 3),    \
        "i"(8 * (S) + 4), "i"(8 * (S) + 7))
#define K2_MM(S) do { K2_MM4(S, 0); K2_MM4(S, 4); } while (0)
#define K2_STEP_A(T, S, NV, ISS)                    \
  do {                                              \
    wait_vm<NV>();                                  \
    WAIT_L0();                                      \
    BAR();                                          \
    if (ISS) K2_ISSUE((T) + 3, ((S) + 3) & 3);      \
    K2_RD(S);                                       \
    K2_MM(S);                                       \
    __builtin_amdgcn_sched_barrier(0);              \
  } while (0)
#define K2_STEP_B(T, S, NV, ISS)                    \
  do {                                              \
    wait_vm<NV>();                                  \
    WAIT_L0();                                      \
    BAR();                                          \
    K2_MM(((S) + 3) & 3);                           \
    __builtin_amdgcn_sched_barrier(0);              \
    if (ISS) K2_ISSUE((T) + 3, ((S) + 3) & 3);      \
    K2_RD(S);                                       \
    __builtin_amdgcn_sched_barrier(0);              \
  } while (0)

  const int rw0 = m0 + ksel * 64, cl0 = n0 + wn * 32;
  u32x2 xv[2], bv[2];
#pragma unroll
  for (int j = 0; j < 2; ++j) {
    xv[j] = *(const u32x2*)(x + (rw0 + sk * 16 + li) * DM + cl0 + 4 * g + j * 16);
    bv[j] = *(const u32x2*)(b2 + cl0 + 4 * g + j * 16);
  }
#pragma unroll
  for (int i = 0; i < 8; ++i) asm volatile("s_nop 1" : "+a"(acc[i][0]), "+a"(acc[i][1]));
  STAMP2(0);
  K2_ISSUE(0, 0);
  K2_ISSUE(1, 1);
  K2_ISSUE(2, 2);
  if (w < 4) {
#pragma unroll 1
    for (int t = 0; t < 20; t += 4) {
      K2_STEP_A(t, 0, 8, true);
      K2_STEP_A(t + 1, 1, 8, true);
      K2_STEP_A(t + 2, 2, 8, true);
      K2_STEP_A(t + 3, 3, 8, true);
    }
    K2_STEP_A(20, 0, 8, false);
    K2_STEP_A(21, 1, 4, false);
    K2_STEP_A(22, 2, 0, false);
  } else {
    wait_vm<8>();
    BAR();
    K2_ISSUE(3, 3);
    K2_RD(0);
    K2_STEP_B(1, 1, 8, true);
    K2_STEP_B(2, 2, 8, true);
    K2_STEP_B(3, 3, 8, true);
#pragma unroll 1
    for (int t = 4; t < 20; t += 4) {
      K2_STEP_B(t, 0, 8, true);
      K2_STEP_B(t + 1, 1, 8, true);
      K2_STEP_B(t + 2, 2, 8, true);
      K2_STEP_B(t + 3, 3, 8, true);
    }
    K2_STEP_B(20, 0, 8, false);
    K2_STEP_B(21, 1, 4, false);
    K2_STEP_B(22, 2, 0, false);
    K2_MM(2);
  }
  STAMP2(5);
  f32x4 r[4][2];
#pragma unroll
  for (int i = 0; i < 8; ++i) asm volatile("s_nop 7" : "+a"(acc[i][0]), "+a"(acc[i][1]));
  WAIT_L0();
  BAR();
  {
    char* mine = smem + w * 8192 + lane * 16;
    const char* theirs = smem + (w ^ 4) * 8192 + lane * 16;
    if (ksel == 0) {
#pragma unroll
      for (int i = 0; i < 4; ++i)
#pragma unroll
        for (int j = 0; j < 2; ++j) { *(f32x4*)(mine + (i * 2 + j) * 1024) = acc[4 + i][j]; r[i][j] = acc[i][j]; }
    } else {
#pragma unroll
      for (int i = 0; i < 4; ++i)
#pragma unroll
        for (int j = 0; j < 2; ++j) { *(f32x4*)(mine + (i * 2 + j) * 1024) = acc[i][j]; r[i][j] = acc[4 + i][j]; }
    }
    WAIT_L0();
    BAR();
    f32x4 o[4][2];
#pragma unroll
    for (int i = 0; i < 4; ++i)
#pragma unroll
      for (int j = 0; j < 2; ++j) o[i][j] = *(const f32x4*)(theirs + (i * 2 + j) * 1024);
#pragma unroll
    for (int i = 0; i < 4; ++i)
#pragma unroll
      for (int j = 0; j < 2; ++j)
#pragma unroll
        for (int q = 0; q < 4; ++q) r[i][j][q] = aadd(r[i][j][q], o[i][j][q]);
  }
  const int tile = mt * 4 + nt;
  if (sk == 0) k2_epi<0>(r, slabs, cnt, x, b2, out, tile, rw0, cl0, w, xv, bv);
  else if (sk == 1) k2_epi<1>(r, slabs, cnt, x, b2, out, tile, rw0, cl0, w, xv, bv);
  else if (sk == 2) k2_epi<2>(r, slabs, cnt, x, b2, out, tile, rw0, cl0, w, xv, bv);
  else k2_epi<3>(r, slabs, cnt, x, b2, out, tile, rw0, cl0, w, xv, bv);
  RSTAMP(1, 31);
}

extern "C" void kernel_launch(void* const* d_in, const int* in_sizes, int n_in, void* d_out, int out_size,
                              void* d_ws, size_t ws_size, hipStream_t stream) {
  const u16* x = (const u16*)d_in[0];
  const u16* Cr = (const u16*)d_in[1];
  const u16* Ci = (const u16*)d_in[2];
  const u16* Wr = (const u16*)d_in[3];
  const u16* Wi = (const u16*)d_in[4];
  const u16* w1 = (const u16*)d_in[6];
  const u16* b1 = (const u16*)d_in[7];
  const u16* w2 = (const u16*)d_in[8];
  const u16* b2 = (const u16*)d_in[9];
  u16* out = (u16*)d_out;
  char* ws = (char*)d_ws;
  u16* BT = (u16*)ws;
  u16* H = (u16*)(ws + 6029312);
  float* slabs = (float*)(ws + 6029312 + 7340032);
  unsigned* cnt = (unsigned*)(ws + 6029312 + 7340032 + 16777216);

  (void)hipFuncSetAttribute((const void*)k1, hipFuncAttributeMaxDynamicSharedMemorySize, K1_LDS);
  (void)hipFuncSetAttribute((const void*)k2, hipFuncAttributeMaxDynamicSharedMemorySize, K2_LDS);
  k1<<<256, 512, K1_LDS, stream>>>(x, Wr, Wi, w1, b1, w2, BT, H, cnt, Cr, Ci);
  k2<<<256, 512, K2_LDS, stream>>>(Cr, Ci, H, BT, x, b2, out, slabs, cnt);
}
```

```cpp
#include <hip/hip_runtime.h>
#include <hip/hip_bf16.h>
#include <stdint.h>

typedef unsigned short u16;
typedef __attribute__((ext_vector_type(8))) short bf16x8;
typedef __attribute__((ext_vector_type(4))) short bf16x4;
typedef __attribute__((ext_vector_type(2))) short bf16x2;
typedef __attribute__((ext_vector_type(4))) float f32x4;
typedef __attribute__((ext_vector_type(4))) unsigned int u32x4;
typedef __attribute__((ext_vector_type(2))) unsigned int u32x2;

#define SEQ 2048
#define DM 512
#define FF 1792
#define KTOT 5888

#define WAIT_V(n) asm volatile("s_waitcnt vmcnt(" #n ")" ::: "memory")
#define WAIT_L0() asm volatile("s_waitcnt lgkmcnt(0)" ::: "memory")
#define BAR() asm volatile("s_barrier" ::: "memory")

__device__ __forceinline__ float bf2f(u16 v) { return __uint_as_float(((unsigned)v) << 16); }
__device__ __forceinline__ float bf_lo(unsigned v) { return __uint_as_float(v << 16); }
__device__ __forceinline__ float bf_hi(unsigned v) { return __uint_as_float(v & 0xffff0000u); }
__device__ __forceinline__ unsigned pack2(float lo, float hi) {
  unsigned r;
  asm("v_cvt_pk_bf16_f32 %0, %1, %2" : "=v"(r) : "v"(lo), "v"(hi));
  return r;
}
__device__ __forceinline__ float rbf(float f) { return bf_lo(pack2(f, f)); }
__device__ __forceinline__ float afma(float a, float b, float c) {
  float r;
  asm("v_fma_f32 %0, %1, %2, %3" : "=v"(r) : "v"(a), "v"(b), "v"(c));
  return r;
}
__device__ __forceinline__ float aadd(float a, float b) {
  float r;
  asm("v_add_f32 %0, %1, %2" : "=v"(r) : "v"(a), "v"(b));
  return r;
}
__device__ __forceinline__ float amul(float a, float b) {
  float r;
  asm("v_mul_f32 %0, %1, %2" : "=v"(r) : "v"(a), "v"(b));
  return r;
}

template <int N> __device__ __forceinline__ void wait_vm() {
  asm volatile("s_waitcnt vmcnt(%0)" ::"i"(N) : "memory");
}

__device__ __forceinline__ void glds16(const void* g, void* l) {
  __builtin_amdgcn_global_load_lds((const __attribute__((address_space(1))) unsigned*)g,
                                   (__attribute__((address_space(3))) unsigned*)l, 16, 0, 0);
}

__device__ constexpr int k1_allowed(int kc, bool pf) {
  return (kc + 2 <= 7 ? 5 : 0) + (pf ? (((kc - 2 >= 1 && ((kc - 2) & 1)) ? 1 : 0) + ((kc - 1 >= 1 && ((kc - 1) & 1)) ? 1 : 0)) : 0);
}
#define K1_CHUNK 38912
#define K1_NBUF 4
#define K1_STATS (K1_NBUF * K1_CHUNK)
#define K1_BIAS (K1_STATS + 1024)
#define K1_LDS (K1_BIAS + 512)

template <int NAB, int WN>
__device__ __forceinline__ void k1_body(const u16* __restrict__ x, const u16* __restrict__ Wr,
                                        const u16* __restrict__ Wi, const u16* __restrict__ w1,
                                        const u16* __restrict__ b1, u16* __restrict__ BT,
                                        u16* __restrict__ H, char* smem, int mt, int nt,
                                        const u16* Cr, const u16* Ci, const u32x4 w2v, int bid) {
  constexpr int NH = 11 - NAB;
  constexpr int ROT = (NH == 11) ? 4 : 1;
  constexpr int NHD = (NH == 0) ? 1 : NH;
  constexpr int NB0 = 6;
  constexpr int wn = WN;
  constexpr int NBW = (wn == 0) ? 6 : 5;
  constexpr bool PF = NAB > 0;
  const int tid = threadIdx.x;
  const int lane = tid & 63;
  const int w = __builtin_amdgcn_readfirstlane(tid >> 6);
  const int wm = w & 3;
  const int li = lane & 15, g = lane >> 4;
  const int c0 = nt * 176;
  const int m0 = mt * 128 + wm * 32;

  const u16* src[5];
  int inc[5], jd[5];
#pragma unroll
  for (int jj = 0; jj < 2; ++jj) {
    const int row = 16 * w + 8 * jj + (lane >> 3);
    const int sw = (((row >> 3) & 3) << 1) | ((row >> 1) & 1);
    src[jj] = x + (mt * 128 + row) * DM + ((lane & 7) ^ sw) * 8;
    inc[jj] = 64;
    jd[jj] = (2 * w + jj) * 1024;
  }
#pragma unroll
  for (int jj = 0; jj < 3; ++jj) {
    int j = w + 8 * jj;
    if (j > 21) j -= 8;
    jd[2 + jj] = 16384 + j * 1024;
    if (NAB == 11 || (NAB > 0 && j < 2 * NAB)) {
      const int row = 8 * j + (lane >> 3);
      const int ch = (lane & 7) ^ ((row >> 1) & 7);
      const int n = c0 + row;
      const u16* base = (n < 512) ? (Wr + n * DM) : (Wi + (n - 512) * DM);
      src[2 + jj] = base + ch * 8;
      inc[2 + jj] = 64;
    } else {
      const unsigned p16 = (j - 2 * NAB) * 64 + lane;
      const unsigned r = p16 / (2 * NHD);
      const unsigned rem = p16 - r * (2 * NHD);
      const int cp = rem >> 1, hf = rem & 1;
      int c = cp - ROT * ((r >> 3) & 1);
      if (c < 0) c += NH;
      src[2 + jj] = w1 + r * FF + (c0 + 16 * NAB - 1024) + 16 * c + 8 * hf;
      inc[2 + jj] = 64 * FF;
    }
  }
  auto issue = [&](int kc) {
#pragma unroll
    for (int jj = 0; jj < 5; ++jj) glds16(src[jj] + kc * inc[jj], smem + (kc % K1_NBUF) * K1_CHUNK + jd[jj]);
  };
  u32x2 bias_v = {0u, 0u};
  if (NH > 0 && tid < NH * 4) bias_v = *(const u32x2*)(b1 + (c0 + 16 * NAB - 1024) + 4 * tid);
  issue(0);
  issue(1);
  issue(2);

  f32x4 acc[2][NBW], cs[NBW];
#pragma unroll
  for (int b = 0; b < NBW; ++b) {
    acc[0][b] = (f32x4){0.f, 0.f, 0.f, 0.f};
    acc[1][b] = (f32x4){0.f, 0.f, 0.f, 0.f};
    cs[b] = (f32x4){0.f, 0.f, 0.f, 0.f};
  }
  f32x4 d1 = {0.f, 0.f, 0.f, 0.f}, d2 = {0.f, 0.f, 0.f, 0.f};
  bf16x8 ones = {0x3f80, 0x3f80, 0x3f80, 0x3f80, 0x3f80, 0x3f80, 0x3f80, 0x3f80};
  asm volatile("" : "+v"(ones));

  const int q4 = li >> 2, p4 = lane & 3;
  const int laneX0 = (wm * 32 + 8 * q4 + (li & 3)) * 128 + ((0 + g) ^ (li >> 1)) * 16;
  const int laneX1 = (wm * 32 + 8 * q4 + (li & 3)) * 128 + ((4 + g) ^ (li >> 1)) * 16;
  const int laneAB0 = 16384 + li * 128 + ((0 + g) ^ (li >> 1)) * 16;
  const int laneAB1 = 16384 + li * 128 + ((4 + g) ^ (li >> 1)) * 16;
  const int laneH0 = 16384 + NAB * 2048 + (8 * g + q4) * (NHD * 32) + (g & 1) * ROT * 32 + p4 * 8;
  const int laneH1 = laneH0 - (g & 1) * NH * 32;
  int boff = 0;
  unsigned pfv[6] = {0u, 0u, 0u, 0u, 0u, 0u};
  auto setbuf = [&](int kc) {
    boff = (kc % K1_NBUF) * K1_CHUNK;
    asm volatile("" : "+s"(boff));
  };
  bf16x8 wf[NBW], xa[2][2];
  auto rdx = [&](int step) {
    const int ks = step & 1;
    xa[ks][0] = *(const bf16x8*)(smem + boff + (ks ? laneX1 : laneX0));
    xa[ks][1] = *(const bf16x8*)(smem + boff + (ks ? laneX1 : laneX0) + 512);
  };
  auto rdw = [&](int step, int bb) {
    const int ks = step & 1;
    const int b = wn * NB0 + bb;
    if (b < NAB) {
      wf[bb] = *(const bf16x8*)(smem + boff + (ks ? laneAB1 : laneAB0) + b * 2048);
    } else {
      const int hb = b - NAB;
      const char* hp = smem + boff + ((hb + ROT >= NH) ? laneH1 : laneH0) + 32 * ks * (NHD * 32) + hb * 32;
      const bf16x4 t0 = __builtin_amdgcn_ds_read_tr16_b64_v4i16((__attribute__((address_space(3))) bf16x4*)(hp));
      const bf16x4 t1 = __builtin_amdgcn_ds_read_tr16_b64_v4i16((__attribute__((address_space(3))) bf16x4*)(hp + 4 * (NHD * 32)));
      wf[bb] = __builtin_shufflevector(t0, t1, 0, 1, 2, 3, 4, 5, 6, 7);
    }
  };
#pragma unroll
  for (int bb = 0; bb < NBW; ++bb) asm volatile("s_nop 1" : "+v"(acc[0][bb]), "+v"(acc[1][bb]), "+v"(cs[bb]));
  asm volatile("s_nop 1" : "+v"(d1), "+v"(d2));
  wait_vm<10>();
  BAR();
  setbuf(0);
  rdx(0);
#pragma unroll
  for (int bb = 0; bb < NBW; ++bb) rdw(0, bb);
#pragma unroll
  for (int step = 0; step < 16; ++step) {
    const int kc = step >> 1, ks = step & 1;
    if (ks && kc < 7) {
      if (kc == 0) wait_vm<k1_allowed(0, PF)>(); else if (kc == 1) wait_vm<k1_allowed(1, PF)>(); else if (kc == 2) wait_vm<k1_allowed(2, PF)>();
      else if (kc == 3) wait_vm<k1_allowed(3, PF)>(); else if (kc == 4) wait_vm<k1_allowed(4, PF)>(); else if (kc == 5) wait_vm<k1_allowed(5, PF)>();
      else wait_vm<k1_allowed(6, PF)>();
      BAR();
      setbuf(kc + 1);
    }
    if (step < 15) rdx(step + 1);
    asm("v_mfma_f32_16x16x32_bf16 %0, %2, %3, %0\n\tv_mfma_f32_16x16x32_bf16 %1, %2, %2, %1"
        : "+v"(d1), "+v"(d2)
        : "v"(xa[ks][wn]), "v"(ones));
    __builtin_amdgcn_sched_barrier(0);
#pragma unroll
    for (int bb = 0; bb < NBW; ++bb) {
      const int b = wn * NB0 + bb;
      if (b < NAB) {
        asm("v_mfma_f32_16x16x32_bf16 %0, %3, %5, %0\n\tv_mfma_f32_16x16x32_bf16 %1, %4, %5, %1\n\t"
            "v_mfma_f32_16x16x32_bf16 %2, %6, %5, %2"
            : "+v"(acc[0][bb]), "+v"(acc[1][bb]), "+v"(cs[bb])
            : "v"(xa[ks][0]), "v"(xa[ks][1]), "v"(wf[bb]), "v"(ones));
      } else {
        asm("v_mfma_f32_16x16x32_bf16 %0, %5, %3, %0\n\tv_mfma_f32_16x16x32_bf16 %1, %5, %4, %1\n\t"
            "v_mfma_f32_16x16x32_bf16 %2, %5, %6, %2"
            : "+v"(acc[0][bb]), "+v"(acc[1][bb]), "+v"(cs[bb])
            : "v"(xa[ks][0]), "v"(xa[ks][1]), "v"(wf[bb]), "v"(ones));
      }
      if (step < 15) rdw(step + 1, bb);
      if (ks && kc < 7 && bb < 5) {
        if (kc + 3 < 8) glds16(src[bb] + (kc + 3) * inc[bb], smem + ((kc + 3) % K1_NBUF) * K1_CHUNK + jd[bb]);
        if (bb == 4) {
          asm volatile("" ::: "memory");
          if (PF && (kc & 1)) {
            unsigned sec = ((kc >> 1) * 96 + mt * 6 + nt) * 512 + tid;
            sec = sec < 131072u ? sec : 131071u;
            pfv[kc - 1] = *(const unsigned*)((sec < 65536u ? (const char*)Cr : (const char*)Ci) + (size_t)(sec & 65535u) * 128);
          }
          asm volatile("" ::: "memory");
        }
      }
      __builtin_amdgcn_sched_barrier(0);
    }
  }

  {
    asm volatile("s_nop 7" : "+v"(d1), "+v"(d2));
    const bool valid = q4 == g;
    const int rr = li & 3;
    const float s = rr == 0 ? d1[0] : rr == 1 ? d1[1] : rr == 2 ? d1[2] : d1[3];
    const float q = rr == 0 ? d2[0] : rr == 1 ? d2[1] : rr == 2 ? d2[2] : d2[3];
    float mean = amul(s, 1.0f / 512.0f);
    float varf = afma(q, 1.0f / 512.0f, -mean * mean);
    const int srow = 8 * q4 + 4 * wn + rr;
    if (__any(valid && amul(mean, mean) > amul(8.0f, varf))) {
      mean = __shfl(mean, q4 * 16 + li);
      float v0 = 0.f, v1 = 0.f;
      for (int ks = 0; ks < 16; ++ks) {
        const u32x4 v = *(const u32x4*)(x + (m0 + srow) * DM + ks * 32 + g * 8);
#pragma unroll
        for (int qq = 0; qq < 4; ++qq) {
          const float e0 = aadd(bf_lo(v[qq]), -mean), e1 = aadd(bf_hi(v[qq]), -mean);
          v0 = afma(e0, e0, v0);
          v1 = afma(e1, e1, v1);
        }
      }
      float vs = v0 + v1;
      vs += __shfl_xor(vs, 16);
      vs += __shfl_xor(vs, 32);
      varf = vs * (1.0f / 512.0f);
    }
    const float var = rbf(varf);
    const float sd = rbf(sqrtf(rbf(var + 1e-5f)));
    const float rinv = 1.0f / sd;
    const float mb16 = rbf(mean);
    if (valid) ((float2*)(smem + K1_STATS))[wm * 32 + srow] = make_float2(rinv, -mb16 * rinv);
    if (NH > 0 && tid < NH * 4) *(u32x2*)(smem + K1_BIAS + tid * 8) = bias_v;
  }
  WAIT_L0();
  BAR();
  if (bid < 224) {
    u16* T = (u16*)(smem + 65536);
    const int f = tid >> 3, e8 = (tid & 7) * 8;
#pragma unroll
    for (int q = 0; q < 4; ++q) {
      T[(e8 + 2 * q) * 72 + f] = (u16)(w2v[q] & 0xffffu);
      T[(e8 + 2 * q + 1) * 72 + f] = (u16)(w2v[q] >> 16);
    }
  }

  const __amdgpu_buffer_rsrc_t rsB = __builtin_amdgcn_make_buffer_rsrc((void*)BT, 0, DM * KTOT * 2, 0x00020000);
  const float2* stp = (const float2*)(smem + K1_STATS) + wm * 32;
  float2 sta[8];
  float2 sth[2];
  if (NAB > wn * NB0) {
#pragma unroll
    for (int i = 0; i < 8; ++i) sta[i] = stp[8 * g + i];
  }
  if (NAB < 11 && !(wn == 0 && NAB >= NB0)) {
#pragma unroll
    for (int mb = 0; mb < 2; ++mb) sth[mb] = stp[8 * q4 + 4 * mb + (li & 3)];
  }
#pragma unroll
  for (int bb = 0; bb < NBW; ++bb) {
    const int b = wn * NB0 + bb;
    if (b < NAB) {
      const int n = c0 + 16 * b + li;
      const bool isb = n >= 512;
      const unsigned sgn = isb ? 0x80008000u : 0u;
      u32x4 o;
#pragma unroll
      for (int mb = 0; mb < 2; ++mb) {
        float t0, t1, t2, t3;
        unsigned o0, o1;
        asm("v_mul_f32 %2, %11, %10\n\tv_mul_f32 %3, %13, %10\n\tv_mul_f32 %4, %15, %10\n\tv_mul_f32 %5, %17, %10\n\t"
            "v_fma_f32 %2, %12, %6, %2\n\tv_fma_f32 %3, %14, %7, %3\n\tv_fma_f32 %4, %16, %8, %4\n\tv_fma_f32 %5, %18, %9, %5\n\t"
            "v_cvt_pk_bf16_f32 %0, %2, %3\n\tv_cvt_pk_bf16_f32 %1, %4, %5\n\t"
            "v_xor_b32 %0, %19, %0\n\tv_xor_b32 %1, %19, %1"
            : "=&v"(o0), "=&v"(o1), "=&v"(t0), "=&v"(t1), "=&v"(t2), "=&v"(t3)
            : "v"(acc[mb][bb][0]), "v"(acc[mb][bb][1]), "v"(acc[mb][bb][2]), "v"(acc[mb][bb][3]), "v"(cs[bb][0]),
              "v"(sta[4 * mb + 0].y), "v"(sta[4 * mb + 0].x), "v"(sta[4 * mb + 1].y), "v"(sta[4 * mb + 1].x),
              "v"(sta[4 * mb + 2].y), "v"(sta[4 * mb + 2].x), "v"(sta[4 * mb + 3].y), "v"(sta[4 * mb + 3].x),
              "v"(sgn));
        o[2 * mb] = o0;
        o[2 * mb + 1] = o1;
      }
      __builtin_amdgcn_raw_buffer_store_b128(o, rsB, ((((n & 511) >> 4) * (KTOT / 32) + (((isb ? 2048 : 0) + m0) >> 5)) * 64 + lane) * 16, 0, 16);
    } else {
      const int fl = 16 * (b - NAB) + 4 * g;
      const u32x2 bv = *(const u32x2*)(smem + K1_BIAS + fl * 2);
      const float bias0 = bf_lo(bv[0]), bias1 = bf_hi(bv[0]), bias2 = bf_lo(bv[1]), bias3 = bf_hi(bv[1]);
#pragma unroll
      for (int mb = 0; mb < 2; ++mb) {
        float t0, t1, t2, t3;
        unsigned o0, o1;
        asm("v_fma_f32 %2, %19, %10, %14\n\tv_fma_f32 %3, %19, %11, %15\n\tv_fma_f32 %4, %19, %12, %16\n\tv_fma_f32 %5, %19, %13, %17\n\t"
            "v_fma_f32 %2, %18, %6, %2\n\tv_fma_f32 %3, %18, %7, %3\n\tv_fma_f32 %4, %18, %8, %4\n\tv_fma_f32 %5, %18, %9, %5\n\t"
            "v_max_f32 %2, 0, %2\n\tv_max_f32 %3, 0, %3\n\tv_max_f32 %4, 0, %4\n\tv_max_f32 %5, 0, %5\n\t"
            "v_mul_f32 %2, %2, %2\n\tv_mul_f32 %3, %3, %3\n\tv_mul_f32 %4, %4, %4\n\tv_mul_f32 %5, %5, %5\n\t"
            "v_cvt_pk_bf16_f32 %0, %2, %3\n\tv_cvt_pk_bf16_f32 %1, %4, %5"
            : "=&v"(o0), "=&v"(o1), "=&v"(t0), "=&v"(t1), "=&v"(t2), "=&v"(t3)
            : "v"(acc[mb][bb][0]), "v"(acc[mb][bb][1]), "v"(acc[mb][bb][2]), "v"(acc[mb][bb][3]),
              "v"(cs[bb][0]), "v"(cs[bb][1]), "v"(cs[bb][2]), "v"(cs[bb][3]),
              "v"(bias0), "v"(bias1), "v"(bias2), "v"(bias3), "v"(sth[mb].x), "v"(sth[mb].y));
        u32x2 o;
        o[0] = o0;
        o[1] = o1;
        const int row = wm * 32 + 8 * q4 + 4 * mb + (li & 3);
        *(u32x2*)(smem + row * (NHD * 32) + fl * 2) = o;
      }
    }
  }
  WAIT_L0();
  BAR();
  if (bid < 224) {
    const int e = tid >> 3, f8 = (tid & 7) * 8;
    const u32x4 v = *(const u32x4*)(smem + 65536 + (e * 72 + f8) * 2);
    const int n = (bid & 7) * 64 + e, k = 4096 + (bid >> 3) * 64 + f8;
    __builtin_amdgcn_raw_buffer_store_b128(v, rsB, (((n >> 4) * (KTOT / 32) + (k >> 5)) * 64 + ((k >> 3) & 3) * 16 + (n & 15)) * 16, 0, 16);
  }
  if (NH > 0) {
    const __amdgpu_buffer_rsrc_t rsH = __builtin_amdgcn_make_buffer_rsrc((void*)H, 0, SEQ * FF * 2, 0x00020000);
    const int hoff = ((mt * 128) * FF + (c0 + 16 * NAB - 1024)) * 2;
#pragma unroll
    for (int i = 0; i < (128 * NHD * 2 + 511) / 512; ++i) {
      const unsigned id = tid + 512 * i;
      if (128 * NHD * 2 % 512 == 0 || id < 128 * NHD * 2) {
        const unsigned row = id / (NHD * 2), c = id - row * (NHD * 2);
        __builtin_amdgcn_raw_buffer_store_b128(*(const u32x4*)(smem + id * 16), rsH, hoff + (row * FF + c * 8) * 2, 0, 16);
      }
    }
  }
  if (PF) asm volatile("" ::"v"(pfv[0]), "v"(pfv[1]), "v"(pfv[2]), "v"(pfv[3]), "v"(pfv[4]), "v"(pfv[5]));
}

__global__ __launch_bounds__(512, 2) void k1(const u16* __restrict__ x, const u16* __restrict__ Wr,
                                             const u16* __restrict__ Wi, const u16* __restrict__ w1,
                                             const u16* __restrict__ b1, const u16* __restrict__ w2,
                                             u16* __restrict__ BT, u16* __restrict__ H,
                                             unsigned* __restrict__ cnt, const u16* __restrict__ Cr,
                                             const u16* __restrict__ Ci) {
  extern __shared__ __attribute__((aligned(16))) char smem[];
  const int bid = blockIdx.x;
  const int xcd = bid & 7, idx = bid >> 3;
  const int mt = (xcd & 1) * 8 + (idx >> 2);
  const int nt = (xcd >> 1) * 4 + (idx & 3);
  const int tid = threadIdx.x;

  const int tf0 = (bid >> 3) * 64, te0 = (bid & 7) * 64;
  u32x4 w2v = {0u, 0u, 0u, 0u};
  if (bid < 224) w2v = *(const u32x4*)(w2 + (tf0 + (tid >> 3)) * DM + te0 + (tid & 7) * 8);
  if (bid == 0 && tid < 64) cnt[tid * 64] = 0u;

  const int wn = __builtin_amdgcn_readfirstlane(tid >> 8);
  if (wn == 0) {
    if (nt <= 4) k1_body<11, 0>(x, Wr, Wi, w1, b1, BT, H, smem, mt, nt, Cr, Ci, w2v, bid);
    else if (nt == 5) k1_body<9, 0>(x, Wr, Wi, w1, b1, BT, H, smem, mt, nt, Cr, Ci, w2v, bid);
    else k1_body<0, 0>(x, Wr, Wi, w1, b1, BT, H, smem, mt, nt, Cr, Ci, w2v, bid);
  } else {
    if (nt <= 4) k1_body<11, 1>(x, Wr, Wi, w1, b1, BT, H, smem, mt, nt, Cr, Ci, w2v, bid);
    else if (nt == 5) k1_body<9, 1>(x, Wr, Wi, w1, b1, BT, H, smem, mt, nt, Cr, Ci, w2v, bid);
    else k1_body<0, 1>(x, Wr, Wi, w1, b1, BT, H, smem, mt, nt, Cr, Ci, w2v, bid);
  }
}

#define K2_NBUF 4
#define K2_TILE 16384
#define K2_LDS 65536
#define K2_NT 23
#define K2_ACLOB "a0", "a1", "a2", "a3", "a4", "a5", "a6", "a7", "a8", "a9", "a10", "a11", "a12", "a13", "a14", "a15", \
                 "a16", "a17", "a18", "a19", "a20", "a21", "a22", "a23", "a24", "a25", "a26", "a27", "a28", "a29", "a30", "a31"

template <int SK>
__device__ __forceinline__ void k2_epi(f32x4 (&r)[4][2], float* __restrict__ slabs, unsigned* __restrict__ cnt,
                                       const u16* __restrict__ x, const u16* __restrict__ b2,
                                       u16* __restrict__ out, int tile, int row0, int col0, int w,
                                       const u32x2 (&xv)[2], const u32x2 (&bv)[2]) {
  const int tid = threadIdx.x, lane = tid & 63;
  const int li = lane & 15, g = lane >> 4;
  const __amdgpu_buffer_rsrc_t rs = __builtin_amdgcn_make_buffer_rsrc((void*)slabs, 0, 64 * 16 * 8192, 0x00020000);
#pragma unroll
  for (int d = 0; d < 4; ++d) {
    if (d == SK) continue;
    const f32x4 p0 = r[d][0], p1 = r[d][1];
    u32x4 o;
    o[0] = pack2(p0[0], p0[1]);
    o[1] = pack2(p0[2], p0[3]);
    o[2] = pack2(p1[0], p1[1]);
    o[3] = pack2(p1[2], p1[3]);
    __builtin_amdgcn_raw_buffer_store_b128(o, rs, ((tile * 4 + d) * 4 + SK) * 8192 + w * 1024 + lane * 16, 0, 16);
  }
  const int row = row0 + SK * 16 + li;
  const int colb = col0 + 4 * g;
  WAIT_V(0);
  __syncthreads();
  if (tid == 0) {
    unsigned* c = cnt + tile * 64;
    __hip_atomic_fetch_add(c, 1u, __ATOMIC_RELAXED, __HIP_MEMORY_SCOPE_AGENT);
    unsigned spins = 0;
    while (__hip_atomic_load(c, __ATOMIC_RELAXED, __HIP_MEMORY_SCOPE_AGENT) < 4u) {
      if (++spins > (1u << 22)) break;
    }
  }
  __syncthreads();
  u32x4 part[4];
#pragma unroll
  for (int s = 0; s < 4; ++s)
    if (s != SK) part[s] = __builtin_amdgcn_raw_buffer_load_b128(rs, ((tile * 4 + SK) * 4 + s) * 8192 + w * 1024 + lane * 16, 0, 16);
#pragma unroll
  for (int j = 0; j < 2; ++j) {
    float sum[4];
#pragma unroll
    for (int i = 0; i < 4; ++i) {
      float t = r[SK][j][i];
#pragma unroll
      for (int s = 0; s < 4; ++s)
        if (s != SK) t = aadd(t, (i & 1) ? bf_hi(part[s][2 * j + (i >> 1)]) : bf_lo(part[s][2 * j + (i >> 1)]));
      sum[i] = t;
    }
    u32x2 o;
    o[0] = pack2(aadd(aadd(bf_lo(xv[j][0]), sum[0]), bf_lo(bv[j][0])), aadd(aadd(bf_hi(xv[j][0]), sum[1]), bf_hi(bv[j][0])));
    o[1] = pack2(aadd(aadd(bf_lo(xv[j][1]), sum[2]), bf_lo(bv[j][1])), aadd(aadd(bf_hi(xv[j][1]), sum[3]), bf_hi(bv[j][1])));
    *(u32x2*)(out + row * DM + colb + j * 16) = o;
  }
}

__global__ __launch_bounds__(512, 2) void k2(const u16* __restrict__ Cr, const u16* __restrict__ Ci,
                                             const u16* __restrict__ H, const u16* __restrict__ BT,
                                             const u16* __restrict__ x, const u16* __restrict__ b2,
                                             u16* __restrict__ out, float* __restrict__ slabs,
                                             unsigned* __restrict__ cnt) {
  extern __shared__ __attribute__((aligned(16))) char smem[];
  const int bid = blockIdx.x;
  const int xcd = bid & 7, idx = bid >> 3;
  const int sk = xcd >> 1;
  const int mt = (xcd & 1) * 8 + (idx >> 2);
  const int nt = idx & 3;
  const int tid = threadIdx.x, lane = tid & 63;
  const int w = __builtin_amdgcn_readfirstlane(tid >> 6);
  const int ksel = w >> 2, wn = w & 3;
  const int li = lane & 15, g = lane >> 4;
  const int m0 = mt * 128, n0 = nt * 128;

  const int row0 = 16 * w + (lane >> 3);
  const int chE = ((lane & 7) ^ (lane >> 4)) * 8, chO = chE ^ 32;
  const int kt0 = sk * K2_NT;
  const char* bsrc = (const char*)BT + ((((n0 >> 4) + 2 * wn) * (KTOT / 32) + 2 * kt0 + ksel) * 64 + lane) * 16;

  f32x4 acc[8][2];
#pragma unroll
  for (int i = 0; i < 8; ++i)
#pragma unroll
    for (int j = 0; j < 2; ++j) acc[i][j] = (f32x4){0.f, 0.f, 0.f, 0.f};
  const int rdA = li * 128 + ((4 * ksel + g) ^ (li >> 1)) * 16;
  bf16x8 af[8];

#define K2_ISSUE(T, S)                                                                                         \
  do {                                                                                                         \
    const int kt_ = kt0 + (T);                                                                                 \
    char* dst_ = smem + (S) * K2_TILE + w * 2048;                                                              \
    const u16* ap_ = (kt_ < 32) ? (Cr + kt_ * 64) : (kt_ < 64) ? (Ci + (kt_ - 32) * 64) : (H + (kt_ - 64) * 64); \
    const int pitch_ = (kt_ < 64) ? 2048 : FF;                                                                 \
    const u16* asrc_ = ap_ + (m0 + row0) * pitch_;                                                             \
    glds16(asrc_ + chE, dst_);                                                                                 \
    glds16(asrc_ + 8 * pitch_ + chO, dst_ + 1024);                                                             \
    const char* bp_ = bsrc + (T) * 2048;                                                                       \
    asm volatile("global_load_dwordx4 a[%c2:%c3], %0, off\n\tglobal_load_dwordx4 a[%c4:%c5], %1, off"          \
                 :                                                                                             \
                 : "v"(bp_), "v"(bp_ + (KTOT / 32) * 1024), "i"(8 * (S)), "i"(8 * (S) + 3), "i"(8 * (S) + 4),  \
                   "i"(8 * (S) + 7)                                                                            \
                 : "memory", K2_ACLOB);                                                                        \
  } while (0)
#define K2_RD(S)                                                                                               \
  do {                                                                                                         \
    _Pragma("unroll") for (int i_ = 0; i_ < 8; ++i_) af[i_] = *(const bf16x8*)(smem + (S) * K2_TILE + rdA + i_ * 2048); \
  } while (0)
#define K2_MM4(S, I)                                                                                           \
  asm volatile("s_nop 1\n\tv_mfma_f32_16x16x32_bf16 %0, a[%c12:%c13], %8, %0\n\tv_mfma_f32_16x16x32_bf16 %1, a[%c14:%c15], %8, %1\n\t" \
      "v_mfma_f32_16x16x32_bf16 %2, a[%c12:%c13], %9, %2\n\tv_mfma_f32_16x16x32_bf16 %3, a[%c14:%c15], %9, %3\n\t"  \
      "v_mfma_f32_16x16x32_bf16 %4, a[%c12:%c13], %10, %4\n\tv_mfma_f32_16x16x32_bf16 %5, a[%c14:%c15], %10, %5\n\t" \
      "v_mfma_f32_16x16x32_bf16 %6, a[%c12:%c13], %11, %6\n\tv_mfma_f32_16x16x32_bf16 %7, a[%c14:%c15], %11, %7"     \
      : "+a"(acc[(I)][0]), "+a"(acc[(I)][1]), "+a"(acc[(I) + 1][0]), "+a"(acc[(I) + 1][1]), "+a"(acc[(I) + 2][0]),  \
        "+a"(acc[(I) + 2][1]), "+a"(acc[(I) + 3][0]), "+a"(acc[(I) + 3][1])                                    \
      : "v"(af[(I)]), "v"(af[(I) + 1]), "v"(af[(I) + 2]), "v"(af[(I) + 3]), "i"(8 * (S)), "i"(8 * (S) + 3),    \
        "i"(8 * (S) + 4), "i"(8 * (S) + 7))
#define K2_MM(S) do { K2_MM4(S, 0); K2_MM4(S, 4); } while (0)
#define K2_STEP_A(T, S, NV, ISS)                    \
  do {                                              \
    wait_vm<NV>();                                  \
    WAIT_L0();                                      \
    BAR();                                          \
    if (ISS) K2_ISSUE((T) + 3, ((S) + 3) & 3);      \
    K2_RD(S);                                       \
    K2_MM(S);                                       \
    __builtin_amdgcn_sched_barrier(0);              \
  } while (0)
#define K2_STEP_B(T, S, NV, ISS)                    \
  do {                                              \
    wait_vm<NV>();                                  \
    WAIT_L0();                                      \
    BAR();                                          \
    K2_MM(((S) + 3) & 3);                           \
    __builtin_amdgcn_sched_barrier(0);              \
    if (ISS) K2_ISSUE((T) + 3, ((S) + 3) & 3);      \
    K2_RD(S);                                       \
    __builtin_amdgcn_sched_barrier(0);              \
  } while (0)

  const int rw0 = m0 + ksel * 64, cl0 = n0 + wn * 32;
  u32x2 xv[2], bv[2];
#pragma unroll
  for (int j = 0; j < 2; ++j) {
    xv[j] = *(const u32x2*)(x + (rw0 + sk * 16 + li) * DM + cl0 + 4 * g + j * 16);
    bv[j] = *(const u32x2*)(b2 + cl0 + 4 * g + j * 16);
  }
#pragma unroll
  for (int i = 0; i < 8; ++i) asm volatile("s_nop 1" : "+a"(acc[i][0]), "+a"(acc[i][1]));
  K2_ISSUE(0, 0);
  K2_ISSUE(1, 1);
  K2_ISSUE(2, 2);
  if (w < 4) {
#pragma unroll 1
    for (int t = 0; t < 20; t += 4) {
      K2_STEP_A(t, 0, 8, true);
      K2_STEP_A(t + 1, 1, 8, true);
      K2_STEP_A(t + 2, 2, 8, true);
      K2_STEP_A(t + 3, 3, 8, true);
    }
    K2_STEP_A(20, 0, 8, false);
    K2_STEP_A(21, 1, 4, false);
    K2_STEP_A(22, 2, 0, false);
  } else {
    wait_vm<8>();
    BAR();
    K2_ISSUE(3, 3);
    K2_RD(0);
    K2_STEP_B(1, 1, 8, true);
    K2_STEP_B(2, 2, 8, true);
    K2_STEP_B(3, 3, 8, true);
#pragma unroll 1
    for (int t = 4; t < 20; t += 4) {
      K2_STEP_B(t, 0, 8, true);
      K2_STEP_B(t + 1, 1, 8, true);
      K2_STEP_B(t + 2, 2, 8, true);
      K2_STEP_B(t + 3, 3, 8, true);
    }
    K2_STEP_B(20, 0, 8, false);
    K2_STEP_B(21, 1, 4, false);
    K2_STEP_B(22, 2, 0, false);
    K2_MM(2);
  }
  f32x4 r[4][2];
#pragma unroll
  for (int i = 0; i < 8; ++i) asm volatile("s_nop 7" : "+a"(acc[i][0]), "+a"(acc[i][1]));
  WAIT_L0();
  BAR();
  {
    char* mine = smem + w * 8192 + lane * 16;
    const char* theirs = smem + (w ^ 4) * 8192 + lane * 16;
    if (ksel == 0) {
#pragma unroll
      for (int i = 0; i < 4; ++i)
#pragma unroll
        for (int j = 0; j < 2; ++j) { *(f32x4*)(mine + (i * 2 + j) * 1024) = acc[4 + i][j]; r[i][j] = acc[i][j]; }
    } else {
#pragma unroll
      for (int i = 0; i < 4; ++i)
#pragma unroll
        for (int j = 0; j < 2; ++j) { *(f32x4*)(mine + (i * 2 + j) * 1024) = acc[i][j]; r[i][j] = acc[4 + i][j]; }
    }
    WAIT_L0();
    BAR();
    f32x4 o[4][2];
#pragma unroll
    for (int i = 0; i < 4; ++i)
#pragma unroll
      for (int j = 0; j < 2; ++j) o[i][j] = *(const f32x4*)(theirs + (i * 2 + j) * 1024);
#pragma unroll
    for (int i = 0; i < 4; ++i)
#pragma unroll
      for (int j = 0; j < 2; ++j)
#pragma unroll
        for (int q = 0; q < 4; ++q) r[i][j][q] = aadd(r[i][j][q], o[i][j][q]);
  }
  const int tile = mt * 4 + nt;
  if (sk == 0) k2_epi<0>(r, slabs, cnt, x, b2, out, tile, rw0, cl0, w, xv, bv);
  else if (sk == 1) k2_epi<1>(r, slabs, cnt, x, b2, out, tile, rw0, cl0, w, xv, bv);
  else if (sk == 2) k2_epi<2>(r, slabs, cnt, x, b2, out, tile, rw0, cl0, w, xv, bv);
  else k2_epi<3>(r, slabs, cnt, x, b2, out, tile, rw0, cl0, w, xv, bv);
}

extern "C" void kernel_launch(void* const* d_in, const int* in_sizes, int n_in, void* d_out, int out_size,
                              void* d_ws, size_t ws_size, hipStream_t stream) {
  const u16* x = (const u16*)d_in[0];
  const u16* Cr = (const u16*)d_in[1];
  const u16* Ci = (const u16*)d_in[2];
  const u16* Wr = (const u16*)d_in[3];
  const u16* Wi = (const u16*)d_in[4];
  const u16* w1 = (const u16*)d_in[6];
  const u16* b1 = (const u16*)d_in[7];
  const u16* w2 = (const u16*)d_in[8];
  const u16* b2 = (const u16*)d_in[9];
  u16* out = (u16*)d_out;
  char* ws = (char*)d_ws;
  u16* BT = (u16*)ws;
  u16* H = (u16*)(ws + 6029312);
  float* slabs = (float*)(ws + 6029312 + 7340032);
  unsigned* cnt = (unsigned*)(ws + 6029312 + 7340032 + 16777216);

  (void)hipFuncSetAttribute((const void*)k1, hipFuncAttributeMaxDynamicSharedMemorySize, K1_LDS);
  (void)hipFuncSetAttribute((const void*)k2, hipFuncAttributeMaxDynamicSharedMemorySize, K2_LDS);
  k1<<<256, 512, K1_LDS, stream>>>(x, Wr, Wi, w1, b1, w2, BT, H, cnt, Cr, Ci);
  k2<<<256, 512, K2_LDS, stream>>>(Cr, Ci, H, BT, x, b2, out, slabs, cnt);
}
```

```cpp
#include <hip/hip_runtime.h>
#include <hip/hip_bf16.h>
#include <stdint.h>

typedef unsigned short u16;
typedef __attribute__((ext_vector_type(8))) short bf16x8;
typedef __attribute__((ext_vector_type(4))) short bf16x4;
typedef __attribute__((ext_vector_type(2))) short bf16x2;
typedef __attribute__((ext_vector_type(4))) float f32x4;
typedef __attribute__((ext_vector_type(4))) unsigned int u32x4;
typedef __attribute__((ext_vector_type(2))) unsigned int u32x2;

#define SEQ 2048
#define DM 512
#define FF 1792
#define KTOT 5888

#define WAIT_V(n) asm volatile("s_waitcnt vmcnt(" #n ")" ::: "memory")
#define WAIT_L0() asm volatile("s_waitcnt lgkmcnt(0)" ::: "memory")
#define BAR() asm volatile("s_barrier" ::: "memory")

__device__ __forceinline__ float bf2f(u16 v) { return __uint_as_float(((unsigned)v) << 16); }
__device__ __forceinline__ float bf_lo(unsigned v) { return __uint_as_float(v << 16); }
__device__ __forceinline__ float bf_hi(unsigned v) { return __uint_as_float(v & 0xffff0000u); }
__device__ __forceinline__ unsigned pack2(float lo, float hi) {
  unsigned r;
  asm("v_cvt_pk_bf16_f32 %0, %1, %2" : "=v"(r) : "v"(lo), "v"(hi));
  return r;
}
__device__ __forceinline__ float rbf(float f) { return bf_lo(pack2(f, f)); }
__device__ __forceinline__ float afma(float a, float b, float c) {
  float r;
  asm("v_fma_f32 %0, %1, %2, %3" : "=v"(r) : "v"(a), "v"(b), "v"(c));
  return r;
}
__device__ __forceinline__ float aadd(float a, float b) {
  float r;
  asm("v_add_f32 %0, %1, %2" : "=v"(r) : "v"(a), "v"(b));
  return r;
}
__device__ __forceinline__ float amul(float a, float b) {
  float r;
  asm("v_mul_f32 %0, %1, %2" : "=v"(r) : "v"(a), "v"(b));
  return r;
}

template <int N> __device__ __forceinline__ void wait_vm() {
  asm volatile("s_waitcnt vmcnt(%0)" ::"i"(N) : "memory");
}

__device__ __forceinline__ void glds16(const void* g, void* l) {
  __builtin_amdgcn_global_load_lds((const __attribute__((address_space(1))) unsigned*)g,
                                   (__attribute__((address_space(3))) unsigned*)l, 16, 0, 0);
}

__device__ constexpr int k1_allowed(int kc, bool pf) {
  return (kc + 2 <= 7 ? 5 : 0) + (pf ? (((kc - 2 >= 1 && ((kc - 2) & 1)) ? 1 : 0) + ((kc - 1 >= 1 && ((kc - 1) & 1)) ? 1 : 0)) : 0);
}
#define K1_CHUNK 38912
#define K1_NBUF 4
#define K1_STATS (K1_NBUF * K1_CHUNK)
#define K1_BIAS (K1_STATS + 1024)
#define K1_LDS (K1_BIAS + 512)

template <int NAB, int WN>
__device__ __forceinline__ void k1_body(const u16* __restrict__ x, const u16* __restrict__ Wr,
                                        const u16* __restrict__ Wi, const u16* __restrict__ w1,
                                        const u16* __restrict__ b1, u16* __restrict__ BT,
                                        u16* __restrict__ H, char* smem, int mt, int nt,
                                        const u16* Cr, const u16* Ci, const u32x4 w2v, int bid) {
  constexpr int NH = 11 - NAB;
  constexpr int ROT = (NH == 11) ? 4 : 1;
  constexpr int NHD = (NH == 0) ? 1 : NH;
  constexpr int NB0 = 6;
  constexpr int wn = WN;
  constexpr int NBW = (wn == 0) ? 6 : 5;
  constexpr bool PF = NAB > 0;
  const int tid = threadIdx.x;
  const int lane = tid & 63;
  const int w = __builtin_amdgcn_readfirstlane(tid >> 6);
  const int wm = w & 3;
  const int li = lane & 15, g = lane >> 4;
  const int c0 = nt * 176;
  const int m0 = mt * 128 + wm * 32;

  const u16* src[5];
  int inc[5], jd[5];
#pragma unroll
  for (int jj = 0; jj < 2; ++jj) {
    const int row = 16 * w + 8 * jj + (lane >> 3);
    const int sw = (((row >> 3) & 3) << 1) | ((row >> 1) & 1);
    src[jj] = x + (mt * 128 + row) * DM + ((lane & 7) ^ sw) * 8;
    inc[jj] = 64;
    jd[jj] = (2 * w + jj) * 1024;
  }
#pragma unroll
  for (int jj = 0; jj < 3; ++jj) {
    int j = w + 8 * jj;
    if (j > 21) j -= 8;
    jd[2 + jj] = 16384 + j * 1024;
    if (NAB == 11 || (NAB > 0 && j < 2 * NAB)) {
      const int row = 8 * j + (lane >> 3);
      const int ch = (lane & 7) ^ ((row >> 1) & 7);
      const int n = c0 + row;
      const u16* base = (n < 512) ? (Wr + n * DM) : (Wi + (n - 512) * DM);
      src[2 + jj] = base + ch * 8;
      inc[2 + jj] = 64;
    } else {
      const unsigned p16 = (j - 2 * NAB) * 64 + lane;
      const unsigned r = p16 / (2 * NHD);
      const unsigned rem = p16 - r * (2 * NHD);
      const int cp = rem >> 1, hf = rem & 1;
      int c = cp - ROT * ((r >> 3) & 1);
      if (c < 0) c += NH;
      src[2 + jj] = w1 + r * FF + (c0 + 16 * NAB - 1024) + 16 * c + 8 * hf;
      inc[2 + jj] = 64 * FF;
    }
  }
  auto issue = [&](int kc) {
#pragma unroll
    for (int jj = 0; jj < 5; ++jj) glds16(src[jj] + kc * inc[jj], smem + (kc % K1_NBUF) * K1_CHUNK + jd[jj]);
  };
  u32x2 bias_v = {0u, 0u};
  if (NH > 0 && tid < NH * 4) bias_v = *(const u32x2*)(b1 + (c0 + 16 * NAB - 1024) + 4 * tid);
  issue(0);
  issue(1);
  issue(2);

  f32x4 acc[2][NBW], cs[NBW];
#pragma unroll
  for (int b = 0; b < NBW; ++b) {
    acc[0][b] = (f32x4){0.f, 0.f, 0.f, 0.f};
    acc[1][b] = (f32x4){0.f, 0.f, 0.f, 0.f};
    cs[b] = (f32x4){0.f, 0.f, 0.f, 0.f};
  }
  f32x4 d1 = {0.f, 0.f, 0.f, 0.f}, d2 = {0.f, 0.f, 0.f, 0.f};
  bf16x8 ones = {0x3f80, 0x3f80, 0x3f80, 0x3f80, 0x3f80, 0x3f80, 0x3f80, 0x3f80};
  asm volatile("" : "+v"(ones));

  const int q4 = li >> 2, p4 = lane & 3;
  const int laneX0 = (wm * 32 + 8 * q4 + (li & 3)) * 128 + ((0 + g) ^ (li >> 1)) * 16;
  const int laneX1 = (wm * 32 + 8 * q4 + (li & 3)) * 128 + ((4 + g) ^ (li >> 1)) * 16;
  const int laneAB0 = 16384 + li * 128 + ((0 + g) ^ (li >> 1)) * 16;
  const int laneAB1 = 16384 + li * 128 + ((4 + g) ^ (li >> 1)) * 16;
  const int laneH0 = 16384 + NAB * 2048 + (8 * g + q4) * (NHD * 32) + (g & 1) * ROT * 32 + p4 * 8;
  const int laneH1 = laneH0 - (g & 1) * NH * 32;
  int boff = 0;
  unsigned pfv[6] = {0u, 0u, 0u, 0u, 0u, 0u};
  auto setbuf = [&](int kc) {
    boff = (kc % K1_NBUF) * K1_CHUNK;
    asm volatile("" : "+s"(boff));
  };
  bf16x8 wf[NBW], xa[2][2];
  auto rdx = [&](int step) {
    const int ks = step & 1;
    xa[ks][0] = *(const bf16x8*)(smem + boff + (ks ? laneX1 : laneX0));
    xa[ks][1] = *(const bf16x8*)(smem + boff + (ks ? laneX1 : laneX0) + 512);
  };
  auto rdw = [&](int step, int bb) {
    const int ks = step & 1;
    const int b = wn * NB0 + bb;
    if (b < NAB) {
      wf[bb] = *(const bf16x8*)(smem + boff + (ks ? laneAB1 : laneAB0) + b * 2048);
    } else {
      const int hb = b - NAB;
      const char* hp = smem + boff + ((hb + ROT >= NH) ? laneH1 : laneH0) + 32 * ks * (NHD * 32) + hb * 32;
      const bf16x4 t0 = __builtin_amdgcn_ds_read_tr16_b64_v4i16((__attribute__((address_space(3))) bf16x4*)(hp));
      const bf16x4 t1 = __builtin_amdgcn_ds_read_tr16_b64_v4i16((__attribute__((address_space(3))) bf16x4*)(hp + 4 * (NHD * 32)));
      wf[bb] = __builtin_shufflevector(t0, t1, 0, 1, 2, 3, 4, 5, 6, 7);
    }
  };
#pragma unroll
  for (int bb = 0; bb < NBW; ++bb) asm volatile("s_nop 1" : "+v"(acc[0][bb]), "+v"(acc[1][bb]), "+v"(cs[bb]));
  asm volatile("s_nop 1" : "+v"(d1), "+v"(d2));
  wait_vm<10>();
  BAR();
  setbuf(0);
  rdx(0);
#pragma unroll
  for (int bb = 0; bb < NBW; ++bb) rdw(0, bb);
#pragma unroll
  for (int step = 0; step < 16; ++step) {
    const int kc = step >> 1, ks = step & 1;
    if (ks && kc < 7) {
      if (kc == 0) wait_vm<k1_allowed(0, PF)>(); else if (kc == 1) wait_vm<k1_allowed(1, PF)>(); else if (kc == 2) wait_vm<k1_allowed(2, PF)>();
      else if (kc == 3) wait_vm<k1_allowed(3, PF)>(); else if (kc == 4) wait_vm<k1_allowed(4, PF)>(); else if (kc == 5) wait_vm<k1_allowed(5, PF)>();
      else wait_vm<k1_allowed(6, PF)>();
      BAR();
      setbuf(kc + 1);
    }
    if (step < 15) rdx(step + 1);
    asm("v_mfma_f32_16x16x32_bf16 %0, %2, %3, %0\n\tv_mfma_f32_16x16x32_bf16 %1, %2, %2, %1"
        : "+v"(d1), "+v"(d2)
        : "v"(xa[ks][wn]), "v"(ones));
    __builtin_amdgcn_sched_barrier(0);
#pragma unroll
    for (int bb = 0; bb < NBW; ++bb) {
      const int b = wn * NB0 + bb;
      if (b < NAB) {
        asm("v_mfma_f32_16x16x32_bf16 %0, %3, %5, %0\n\tv_mfma_f32_16x16x32_bf16 %1, %4, %5, %1\n\t"
            "v_mfma_f32_16x16x32_bf16 %2, %6, %5, %2"
            : "+v"(acc[0][bb]), "+v"(acc[1][bb]), "+v"(cs[bb])
            : "v"(xa[ks][0]), "v"(xa[ks][1]), "v"(wf[bb]), "v"(ones));
      } else {
        asm("v_mfma_f32_16x16x32_bf16 %0, %5, %3, %0\n\tv_mfma_f32_16x16x32_bf16 %1, %5, %4, %1\n\t"
            "v_mfma_f32_16x16x32_bf16 %2, %5, %6, %2"
            : "+v"(acc[0][bb]), "+v"(acc[1][bb]), "+v"(cs[bb])
            : "v"(xa[ks][0]), "v"(xa[ks][1]), "v"(wf[bb]), "v"(ones));
      }
      if (step < 15) rdw(step + 1, bb);
      if (ks && kc < 7 && bb < 5) {
        if (kc + 3 < 8) glds16(src[bb] + (kc + 3) * inc[bb], smem + ((kc + 3) % K1_NBUF) * K1_CHUNK + jd[bb]);
        if (bb == 4) {
          asm volatile("" ::: "memory");
          if (PF && (kc & 1)) {
            unsigned sec = ((kc >> 1) * 96 + mt * 6 + nt) * 512 + tid;
            sec = sec < 131072u ? sec : 131071u;
            pfv[kc - 1] = *(const unsigned*)((sec < 65536u ? (const char*)Cr : (const char*)Ci) + (size_t)(sec & 65535u) * 128);
          }
          asm volatile("" ::: "memory");
        }
      }
      __builtin_amdgcn_sched_barrier(0);
    }
  }

  {
    asm volatile("s_nop 7" : "+v"(d1), "+v"(d2));
    const bool valid = q4 == g;
    const int rr = li & 3;
    const float s = rr == 0 ? d1[0] : rr == 1 ? d1[1] : rr == 2 ? d1[2] : d1[3];
    const float q = rr == 0 ? d2[0] : rr == 1 ? d2[1] : rr == 2 ? d2[2] : d2[3];
    float mean = amul(s, 1.0f / 512.0f);
    float varf = afma(q, 1.0f / 512.0f, -mean * mean);
    const int srow = 8 * q4 + 4 * wn + rr;
    if (__any(valid && amul(mean, mean) > amul(8.0f, varf))) {
      mean = __shfl(mean, q4 * 16 + li);
      float v0 = 0.f, v1 = 0.f;
      for (int ks = 0; ks < 16; ++ks) {
        const u32x4 v = *(const u32x4*)(x + (m0 + srow) * DM + ks * 32 + g * 8);
#pragma unroll
        for (int qq = 0; qq < 4; ++qq) {
          const float e0 = aadd(bf_lo(v[qq]), -mean), e1 = aadd(bf_hi(v[qq]), -mean);
          v0 = afma(e0, e0, v0);
          v1 = afma(e1, e1, v1);
        }
      }
      float vs = v0 + v1;
      vs += __shfl_xor(vs, 16);
      vs += __shfl_xor(vs, 32);
      varf = vs * (1.0f / 512.0f);
    }
    const float var = rbf(varf);
    const float sd = rbf(sqrtf(rbf(var + 1e-5f)));
    const float rinv = 1.0f / sd;
    const float mb16 = rbf(mean);
    if (valid) ((float2*)(smem + K1_STATS))[wm * 32 + srow] = make_float2(rinv, -mb16 * rinv);
    if (NH > 0 && tid < NH * 4) *(u32x2*)(smem + K1_BIAS + tid * 8) = bias_v;
  }
  WAIT_L0();
  BAR();
  if (bid < 224) {
    u16* T = (u16*)(smem + 65536);
    const int f = tid >> 3, e8 = (tid & 7) * 8;
#pragma unroll
    for (int q = 0; q < 4; ++q) {
      T[(e8 + 2 * q) * 72 + f] = (u16)(w2v[q] & 0xffffu);
      T[(e8 + 2 * q + 1) * 72 + f] = (u16)(w2v[q] >> 16);
    }
  }

  const __amdgpu_buffer_rsrc_t rsB = __builtin_amdgcn_make_buffer_rsrc((void*)BT, 0, DM * KTOT * 2, 0x00020000);
  const float2* stp = (const float2*)(smem + K1_STATS) + wm * 32;
  float2 sta[8];
  float2 sth[2];
  if (NAB > wn * NB0) {
#pragma unroll
    for (int i = 0; i < 8; ++i) sta[i] = stp[8 * g + i];
  }
  if (NAB < 11 && !(wn == 0 && NAB >= NB0)) {
#pragma unroll
    for (int mb = 0; mb < 2; ++mb) sth[mb] = stp[8 * q4 + 4 * mb + (li & 3)];
  }
#pragma unroll
  for (int bb = 0; bb < NBW; ++bb) {
    const int b = wn * NB0 + bb;
    if (b < NAB) {
      const int n = c0 + 16 * b + li;
      const bool isb = n >= 512;
      const unsigned sgn = isb ? 0x80008000u : 0u;
      u32x4 o;
#pragma unroll
      for (int mb = 0; mb < 2; ++mb) {
        float t0, t1, t2, t3;
        unsigned o0, o1;
        asm("v_mul_f32 %2, %11, %10\n\tv_mul_f32 %3, %13, %10\n\tv_mul_f32 %4, %15, %10\n\tv_mul_f32 %5, %17, %10\n\t"
            "v_fma_f32 %2, %12, %6, %2\n\tv_fma_f32 %3, %14, %7, %3\n\tv_fma_f32 %4, %16, %8, %4\n\tv_fma_f32 %5, %18, %9, %5\n\t"
            "v_cvt_pk_bf16_f32 %0, %2, %3\n\tv_cvt_pk_bf16_f32 %1, %4, %5\n\t"
            "v_xor_b32 %0, %19, %0\n\tv_xor_b32 %1, %19, %1"
            : "=&v"(o0), "=&v"(o1), "=&v"(t0), "=&v"(t1), "=&v"(t2), "=&v"(t3)
            : "v"(acc[mb][bb][0]), "v"(acc[mb][bb][1]), "v"(acc[mb][bb][2]), "v"(acc[mb][bb][3]), "v"(cs[bb][0]),
              "v"(sta[4 * mb + 0].y), "v"(sta[4 * mb + 0].x), "v"(sta[4 * mb + 1].y), "v"(sta[4 * mb + 1].x),
              "v"(sta[4 * mb + 2].y), "v"(sta[4 * mb + 2].x), "v"(sta[4 * mb + 3].y), "v"(sta[4 * mb + 3].x),
              "v"(sgn));
        o[2 * mb] = o0;
        o[2 * mb + 1] = o1;
      }
      __builtin_amdgcn_raw_buffer_store_b128(o, rsB, ((((n & 511) >> 4) * (KTOT / 32) + (((isb ? 2048 : 0) + m0) >> 5)) * 64 + lane) * 16, 0, 16);
    } else {
      const int fl = 16 * (b - NAB) + 4 * g;
      const u32x2 bv = *(const u32x2*)(smem + K1_BIAS + fl * 2);
      const float bias0 = bf_lo(bv[0]), bias1 = bf_hi(bv[0]), bias2 = bf_lo(bv[1]), bias3 = bf_hi(bv[1]);
#pragma unroll
      for (int mb = 0; mb < 2; ++mb) {
        float t0, t1, t2, t3;
        unsigned o0, o1;
        asm("v_fma_f32 %2, %19, %10, %14\n\tv_fma_f32 %3, %19, %11, %15\n\tv_fma_f32 %4, %19, %12, %16\n\tv_fma_f32 %5, %19, %13, %17\n\t"
            "v_fma_f32 %2, %18, %6, %2\n\tv_fma_f32 %3, %18, %7, %3\n\tv_fma_f32 %4, %18, %8, %4\n\tv_fma_f32 %5, %18, %9, %5\n\t"
            "v_max_f32 %2, 0, %2\n\tv_max_f32 %3, 0, %3\n\tv_max_f32 %4, 0, %4\n\tv_max_f32 %5, 0, %5\n\t"
            "v_mul_f32 %2, %2, %2\n\tv_mul_f32 %3, %3, %3\n\tv_mul_f32 %4, %4, %4\n\tv_mul_f32 %5, %5, %5\n\t"
            "v_cvt_pk_bf16_f32 %0, %2, %3\n\tv_cvt_pk_bf16_f32 %1, %4, %5"
            : "=&v"(o0), "=&v"(o1), "=&v"(t0), "=&v"(t1), "=&v"(t2), "=&v"(t3)
            : "v"(acc[mb][bb][0]), "v"(acc[mb][bb][1]), "v"(acc[mb][bb][2]), "v"(acc[mb][bb][3]),
              "v"(cs[bb][0]), "v"(cs[bb][1]), "v"(cs[bb][2]), "v"(cs[bb][3]),
              "v"(bias0), "v"(bias1), "v"(bias2), "v"(bias3), "v"(sth[mb].x), "v"(sth[mb].y));
        u32x2 o;
        o[0] = o0;
        o[1] = o1;
        const int row = wm * 32 + 8 * q4 + 4 * mb + (li & 3);
        *(u32x2*)(smem + row * (NHD * 32) + fl * 2) = o;
      }
    }
  }
  WAIT_L0();
  BAR();
  if (bid < 224) {
    const int e = tid >> 3, f8 = (tid & 7) * 8;
    const u32x4 v = *(const u32x4*)(smem + 65536 + (e * 72 + f8) * 2);
    const int n = (bid & 7) * 64 + e, k = 4096 + (bid >> 3) * 64 + f8;
    __builtin_amdgcn_raw_buffer_store_b128(v, rsB, (((n >> 4) * (KTOT / 32) + (k >> 5)) * 64 + ((k >> 3) & 3) * 16 + (n & 15)) * 16, 0, 16);
  }
  if (NH > 0) {
    const __amdgpu_buffer_rsrc_t rsH = __builtin_amdgcn_make_buffer_rsrc((void*)H, 0, SEQ * FF * 2, 0x00020000);
    const int hoff = ((mt * 128) * FF + (c0 + 16 * NAB - 1024)) * 2;
#pragma unroll
    for (int i = 0; i < (128 * NHD * 2 + 511) / 512; ++i) {
      const unsigned id = tid + 512 * i;
      if (128 * NHD * 2 % 512 == 0 || id < 128 * NHD * 2) {
        const unsigned row = id / (NHD * 2), c = id - row * (NHD * 2);
        __builtin_amdgcn_raw_buffer_store_b128(*(const u32x4*)(smem + id * 16), rsH, hoff + (row * FF + c * 8) * 2, 0, 16);
      }
    }
  }
  if (PF) asm volatile("" ::"v"(pfv[0]), "v"(pfv[1]), "v"(pfv[2]), "v"(pfv[3]), "v"(pfv[4]), "v"(pfv[5]));
}

extern "C" __global__ __launch_bounds__(512, 2) void fnet_k1(const u16* __restrict__ x, const u16* __restrict__ Wr,
                                             const u16* __restrict__ Wi, const u16* __restrict__ w1,
                                             const u16* __restrict__ b1, const u16* __restrict__ w2,
                                             u16* __restrict__ BT, u16* __restrict__ H,
                                             unsigned* __restrict__ cnt, const u16* __restrict__ Cr,
                                             const u16* __restrict__ Ci) {
  extern __shared__ __attribute__((aligned(16))) char smem[];
  const int bid = blockIdx.x;
  const int xcd = bid & 7, idx = bid >> 3;
  const int mt = (xcd & 1) * 8 + (idx >> 2);
  const int nt = (xcd >> 1) * 4 + (idx & 3);
  const int tid = threadIdx.x;

  unsigned cpf = 0;
  if (idx < 2) {
    unsigned alo, ahi, elo, ehi;
    asm volatile("s_getpc_b64 s[20:21]\n\ts_add_u32 %0, s20, fnet_k1@rel32@lo+4\n\ts_addc_u32 %1, s21, fnet_k1@rel32@hi+12"
                 : "=s"(alo), "=s"(ahi) : : "s20", "s21", "scc");
    asm volatile("s_getpc_b64 s[20:21]\n\ts_add_u32 %0, s20, fnet_end@rel32@lo+4\n\ts_addc_u32 %1, s21, fnet_end@rel32@hi+12"
                 : "=s"(elo), "=s"(ehi) : : "s20", "s21", "scc");
    const unsigned long long a0 = ((unsigned long long)ahi << 32) | alo, e0 = ((unsigned long long)ehi << 32) | elo;
    const unsigned long long off = (unsigned long long)(idx * 512 + tid) * 128;
    if (e0 > a0 && a0 + off < e0) cpf = *(const __attribute__((address_space(1))) unsigned*)(a0 + off);
  }
  const int tf0 = (bid >> 3) * 64, te0 = (bid & 7) * 64;
  u32x4 w2v = {0u, 0u, 0u, 0u};
  if (bid < 224) w2v = *(const u32x4*)(w2 + (tf0 + (tid >> 3)) * DM + te0 + (tid & 7) * 8);
  if (bid == 0 && tid < 64) cnt[tid * 64] = 0u;

  const int wn = __builtin_amdgcn_readfirstlane(tid >> 8);
  if (wn == 0) {
    if (nt <= 4) k1_body<11, 0>(x, Wr, Wi, w1, b1, BT, H, smem, mt, nt, Cr, Ci, w2v, bid);
    else if (nt == 5) k1_body<9, 0>(x, Wr, Wi, w1, b1, BT, H, smem, mt, nt, Cr, Ci, w2v, bid);
    else k1_body<0, 0>(x, Wr, Wi, w1, b1, BT, H, smem, mt, nt, Cr, Ci, w2v, bid);
  } else {
    if (nt <= 4) k1_body<11, 1>(x, Wr, Wi, w1, b1, BT, H, smem, mt, nt, Cr, Ci, w2v, bid);
    else if (nt == 5) k1_body<9, 1>(x, Wr, Wi, w1, b1, BT, H, smem, mt, nt, Cr, Ci, w2v, bid);
    else k1_body<0, 1>(x, Wr, Wi, w1, b1, BT, H, smem, mt, nt, Cr, Ci, w2v, bid);
  }
  asm volatile("" ::"v"(cpf));
}

#define K2_NBUF 4
#define K2_TILE 16384
#define K2_LDS 65536
#define K2_NT 23
#define K2_ACLOB "a0", "a1", "a2", "a3", "a4", "a5", "a6", "a7", "a8", "a9", "a10", "a11", "a12", "a13", "a14", "a15", \
                 "a16", "a17", "a18", "a19", "a20", "a21", "a22", "a23", "a24", "a25", "a26", "a27", "a28", "a29", "a30", "a31"

template <int SK>
__device__ __forceinline__ void k2_epi(f32x4 (&r)[4][2], float* __restrict__ slabs, unsigned* __restrict__ cnt,
                                       const u16* __restrict__ x, const u16* __restrict__ b2,
                                       u16* __restrict__ out, int tile, int row0, int col0, int w,
                                       const u32x2 (&xv)[2], const u32x2 (&bv)[2]) {
  const int tid = threadIdx.x, lane = tid & 63;
  const int li = lane & 15, g = lane >> 4;
  const __amdgpu_buffer_rsrc_t rs = __builtin_amdgcn_make_buffer_rsrc((void*)slabs, 0, 64 * 16 * 8192, 0x00020000);
#pragma unroll
  for (int d = 0; d < 4; ++d) {
    if (d == SK) continue;
    const f32x4 p0 = r[d][0], p1 = r[d][1];
    u32x4 o;
    o[0] = pack2(p0[0], p0[1]);
    o[1] = pack2(p0[2], p0[3]);
    o[2] = pack2(p1[0], p1[1]);
    o[3] = pack2(p1[2], p1[3]);
    __builtin_amdgcn_raw_buffer_store_b128(o, rs, ((tile * 4 + d) * 4 + SK) * 8192 + w * 1024 + lane * 16, 0, 16);
  }
  const int row = row0 + SK * 16 + li;
  const int colb = col0 + 4 * g;
  WAIT_V(0);
  __syncthreads();
  if (tid == 0) {
    unsigned* c = cnt + tile * 64;
    __hip_atomic_fetch_add(c, 1u, __ATOMIC_RELAXED, __HIP_MEMORY_SCOPE_AGENT);
    unsigned spins = 0;
    while (__hip_atomic_load(c, __ATOMIC_RELAXED, __HIP_MEMORY_SCOPE_AGENT) < 4u) {
      if (++spins > (1u << 22)) break;
    }
  }
  __syncthreads();
  u32x4 part[4];
#pragma unroll
  for (int s = 0; s < 4; ++s)
    if (s != SK) part[s] = __builtin_amdgcn_raw_buffer_load_b128(rs, ((tile * 4 + SK) * 4 + s) * 8192 + w * 1024 + lane * 16, 0, 16);
#pragma unroll
  for (int j = 0; j < 2; ++j) {
    float sum[4];
#pragma unroll
    for (int i = 0; i < 4; ++i) {
      float t = r[SK][j][i];
#pragma unroll
      for (int s = 0; s < 4; ++s)
        if (s != SK) t = aadd(t, (i & 1) ? bf_hi(part[s][2 * j + (i >> 1)]) : bf_lo(part[s][2 * j + (i >> 1)]));
      sum[i] = t;
    }
    u32x2 o;
    o[0] = pack2(aadd(aadd(bf_lo(xv[j][0]), sum[0]), bf_lo(bv[j][0])), aadd(aadd(bf_hi(xv[j][0]), sum[1]), bf_hi(bv[j][0])));
    o[1] = pack2(aadd(aadd(bf_lo(xv[j][1]), sum[2]), bf_lo(bv[j][1])), aadd(aadd(bf_hi(xv[j][1]), sum[3]), bf_hi(bv[j][1])));
    *(u32x2*)(out + row * DM + colb + j * 16) = o;
  }
}

extern "C" __global__ __launch_bounds__(512, 2) void fnet_k2(const u16* __restrict__ Cr, const u16* __restrict__ Ci,
                                             const u16* __restrict__ H, const u16* __restrict__ BT,
                                             const u16* __restrict__ x, const u16* __restrict__ b2,
                                             u16* __restrict__ out, float* __restrict__ slabs,
                                             unsigned* __restrict__ cnt) {
  extern __shared__ __attribute__((aligned(16))) char smem[];
  const int bid = blockIdx.x;
  const int xcd = bid & 7, idx = bid >> 3;
  const int sk = xcd >> 1;
  const int mt = (xcd & 1) * 8 + (idx >> 2);
  const int nt = idx & 3;
  const int tid = threadIdx.x, lane = tid & 63;
  const int w = __builtin_amdgcn_readfirstlane(tid >> 6);
  const int ksel = w >> 2, wn = w & 3;
  const int li = lane & 15, g = lane >> 4;
  const int m0 = mt * 128, n0 = nt * 128;

  unsigned cpf = 0;
  if (idx == 0) {
    unsigned alo, ahi, elo, ehi;
    asm volatile("s_getpc_b64 s[20:21]\n\ts_add_u32 %0, s20, fnet_k2@rel32@lo+4\n\ts_addc_u32 %1, s21, fnet_k2@rel32@hi+12"
                 : "=s"(alo), "=s"(ahi) : : "s20", "s21", "scc");
    asm volatile("s_getpc_b64 s[20:21]\n\ts_add_u32 %0, s20, fnet_end@rel32@lo+4\n\ts_addc_u32 %1, s21, fnet_end@rel32@hi+12"
                 : "=s"(elo), "=s"(ehi) : : "s20", "s21", "scc");
    const unsigned long long a0 = ((unsigned long long)ahi << 32) | alo, e0 = ((unsigned long long)ehi << 32) | elo;
    const unsigned long long off = (unsigned long long)tid * 128;
    if (e0 > a0 && a0 + off < e0) cpf = *(const __attribute__((address_space(1))) unsigned*)(a0 + off);
  }
  const int row0 = 16 * w + (lane >> 3);
  const int chE = ((lane & 7) ^ (lane >> 4)) * 8, chO = chE ^ 32;
  const int kt0 = sk * K2_NT;
  const char* bsrc = (const char*)BT + ((((n0 >> 4) + 2 * wn) * (KTOT / 32) + 2 * kt0 + ksel) * 64 + lane) * 16;

  f32x4 acc[8][2];
#pragma unroll
  for (int i = 0; i < 8; ++i)
#pragma unroll
    for (int j = 0; j < 2; ++j) acc[i][j] = (f32x4){0.f, 0.f, 0.f, 0.f};
  const int rdA = li * 128 + ((4 * ksel + g) ^ (li >> 1)) * 16;
  bf16x8 af[8];

#define K2_ISSUE(T, S)                                                                                         \
  do {                                                                                                         \
    const int kt_ = kt0 + (T);                                                                                 \
    char* dst_ = smem + (S) * K2_TILE + w * 2048;                                                              \
    const u16* ap_ = (kt_ < 32) ? (Cr + kt_ * 64) : (kt_ < 64) ? (Ci + (kt_ - 32) * 64) : (H + (kt_ - 64) * 64); \
    const int pitch_ = (kt_ < 64) ? 2048 : FF;                                                                 \
    const u16* asrc_ = ap_ + (m0 + row0) * pitch_;                                                             \
    glds16(asrc_ + chE, dst_);                                                                                 \
    glds16(asrc_ + 8 * pitch_ + chO, dst_ + 1024);                                                             \
    const char* bp_ = bsrc + (T) * 2048;                                                                       \
    asm volatile("global_load_dwordx4 a[%c2:%c3], %0, off\n\tglobal_load_dwordx4 a[%c4:%c5], %1, off"          \
                 :                                                                                             \
                 : "v"(bp_), "v"(bp_ + (KTOT / 32) * 1024), "i"(8 * (S)), "i"(8 * (S) + 3), "i"(8 * (S) + 4),  \
                   "i"(8 * (S) + 7)                                                                            \
                 : "memory", K2_ACLOB);                                                                        \
  } while (0)
#define K2_RD(S)                                                                                               \
  do {                                                                                                         \
    _Pragma("unroll") for (int i_ = 0; i_ < 8; ++i_) af[i_] = *(const bf16x8*)(smem + (S) * K2_TILE + rdA + i_ * 2048); \
  } while (0)
#define K2_MM4(S, I)                                                                                           \
  asm volatile("s_nop 1\n\tv_mfma_f32_16x16x32_bf16 %0, a[%c12:%c13], %8, %0\n\tv_mfma_f32_16x16x32_bf16 %1, a[%c14:%c15], %8, %1\n\t" \
      "v_mfma_f32_16x16x32_bf16 %2, a[%c12:%c13], %9, %2\n\tv_mfma_f32_16x16x32_bf16 %3, a[%c14:%c15], %9, %3\n\t"  \
      "v_mfma_f32_16x16x32_bf16 %4, a[%c12:%c13], %10, %4\n\tv_mfma_f32_16x16x32_bf16 %5, a[%c14:%c15], %10, %5\n\t" \
      "v_mfma_f32_16x16x32_bf16 %6, a[%c12:%c13], %11, %6\n\tv_mfma_f32_16x16x32_bf16 %7, a[%c14:%c15], %11, %7"     \
      : "+a"(acc[(I)][0]), "+a"(acc[(I)][1]), "+a"(acc[(I) + 1][0]), "+a"(acc[(I) + 1][1]), "+a"(acc[(I) + 2][0]),  \
        "+a"(acc[(I) + 2][1]), "+a"(acc[(I) + 3][0]), "+a"(acc[(I) + 3][1])                                    \
      : "v"(af[(I)]), "v"(af[(I) + 1]), "v"(af[(I) + 2]), "v"(af[(I) + 3]), "i"(8 * (S)), "i"(8 * (S) + 3),    \
        "i"(8 * (S) + 4), "i"(8 * (S) + 7))
#define K2_MM(S) do { K2_MM4(S, 0); K2_MM4(S, 4); } while (0)
#define K2_STEP_A(T, S, NV, ISS)                    \
  do {                                              \
    wait_vm<NV>();                                  \
    WAIT_L0();                                      \
    BAR();                                          \
    if (ISS) K2_ISSUE((T) + 3, ((S) + 3) & 3);      \
    K2_RD(S);                                       \
    K2_MM(S);                                       \
    __builtin_amdgcn_sched_barrier(0);              \
  } while (0)
#define K2_STEP_B(T, S, NV, ISS)                    \
  do {                                              \
    wait_vm<NV>();                                  \
    WAIT_L0();                                      \
    BAR();                                          \
    K2_MM(((S) + 3) & 3);                           \
    __builtin_amdgcn_sched_barrier(0);              \
    if (ISS) K2_ISSUE((T) + 3, ((S) + 3) & 3);      \
    K2_RD(S);                                       \
    __builtin_amdgcn_sched_barrier(0);              \
  } while (0)

  const int rw0 = m0 + ksel * 64, cl0 = n0 + wn * 32;
  u32x2 xv[2], bv[2];
#pragma unroll
  for (int j = 0; j < 2; ++j) {
    xv[j] = *(const u32x2*)(x + (rw0 + sk * 16 + li) * DM + cl0 + 4 * g + j * 16);
    bv[j] = *(const u32x2*)(b2 + cl0 + 4 * g + j * 16);
  }
#pragma unroll
  for (int i = 0; i < 8; ++i) asm volatile("s_nop 1" : "+a"(acc[i][0]), "+a"(acc[i][1]));
  K2_ISSUE(0, 0);
  K2_ISSUE(1, 1);
  K2_ISSUE(2, 2);
  if (w < 4) {
#pragma unroll 1
    for (int t = 0; t < 20; t += 4) {
      K2_STEP_A(t, 0, 8, true);
      K2_STEP_A(t + 1, 1, 8, true);
      K2_STEP_A(t + 2, 2, 8, true);
      K2_STEP_A(t + 3, 3, 8, true);
    }
    K2_STEP_A(20, 0, 8, false);
    K2_STEP_A(21, 1, 4, false);
    K2_STEP_A(22, 2, 0, false);
  } else {
    wait_vm<8>();
    BAR();
    K2_ISSUE(3, 3);
    K2_RD(0);
    K2_STEP_B(1, 1, 8, true);
    K2_STEP_B(2, 2, 8, true);
    K2_STEP_B(3, 3, 8, true);
#pragma unroll 1
    for (int t = 4; t < 20; t += 4) {
      K2_STEP_B(t, 0, 8, true);
      K2_STEP_B(t + 1, 1, 8, true);
      K2_STEP_B(t + 2, 2, 8, true);
      K2_STEP_B(t + 3, 3, 8, true);
    }
    K2_STEP_B(20, 0, 8, false);
    K2_STEP_B(21, 1, 4, false);
    K2_STEP_B(22, 2, 0, false);
    K2_MM(2);
  }
  f32x4 r[4][2];
#pragma unroll
  for (int i = 0; i < 8; ++i) asm volatile("s_nop 7" : "+a"(acc[i][0]), "+a"(acc[i][1]));
  WAIT_L0();
  BAR();
  {
    char* mine = smem + w * 8192 + lane * 16;
    const char* theirs = smem + (w ^ 4) * 8192 + lane * 16;
    if (ksel == 0) {
#pragma unroll
      for (int i = 0; i < 4; ++i)
#pragma unroll
        for (int j = 0; j < 2; ++j) { *(f32x4*)(mine + (i * 2 + j) * 1024) = acc[4 + i][j]; r[i][j] = acc[i][j]; }
    } else {
#pragma unroll
      for (int i = 0; i < 4; ++i)
#pragma unroll
        for (int j = 0; j < 2; ++j) { *(f32x4*)(mine + (i * 2 + j) * 1024) = acc[i][j]; r[i][j] = acc[4 + i][j]; }
    }
    WAIT_L0();
    BAR();
    f32x4 o[4][2];
#pragma unroll
    for (int i = 0; i < 4; ++i)
#pragma unroll
      for (int j = 0; j < 2; ++j) o[i][j] = *(const f32x4*)(theirs + (i * 2 + j) * 1024);
#pragma unroll
    for (int i = 0; i < 4; ++i)
#pragma unroll
      for (int j = 0; j < 2; ++j)
#pragma unroll
        for (int q = 0; q < 4; ++q) r[i][j][q] = aadd(r[i][j][q], o[i][j][q]);
  }
  const int tile = mt * 4 + nt;
  if (sk == 0) k2_epi<0>(r, slabs, cnt, x, b2, out, tile, rw0, cl0, w, xv, bv);
  else if (sk == 1) k2_epi<1>(r, slabs, cnt, x, b2, out, tile, rw0, cl0, w, xv, bv);
  else if (sk == 2) k2_epi<2>(r, slabs, cnt, x, b2, out, tile, rw0, cl0, w, xv, bv);
  else k2_epi<3>(r, slabs, cnt, x, b2, out, tile, rw0, cl0, w, xv, bv);
  asm volatile("" ::"v"(cpf));
}

extern "C" __global__ void fnet_end() {}

extern "C" void kernel_launch(void* const* d_in, const int* in_sizes, int n_in, void* d_out, int out_size,
                              void* d_ws, size_t ws_size, hipStream_t stream) {
  const u16* x = (const u16*)d_in[0];
  const u16* Cr = (const u16*)d_in[1];
  const u16* Ci = (const u16*)d_in[2];
  const u16* Wr = (const u16*)d_in[3];
  const u16* Wi = (const u16*)d_in[4];
  const u16* w1 = (const u16*)d_in[6];
  const u16* b1 = (const u16*)d_in[7];
  const u16* w2 = (const u16*)d_in[8];
  const u16* b2 = (const u16*)d_in[9];
  u16* out = (u16*)d_out;
  char* ws = (char*)d_ws;
  u16* BT = (u16*)ws;
  u16* H = (u16*)(ws + 6029312);
  float* slabs = (float*)(ws + 6029312 + 7340032);
  unsigned* cnt = (unsigned*)(ws + 6029312 + 7340032 + 16777216);

  (void)hipFuncSetAttribute((const void*)fnet_k1, hipFuncAttributeMaxDynamicSharedMemorySize, K1_LDS);
  (void)hipFuncSetAttribute((const void*)fnet_k2, hipFuncAttributeMaxDynamicSharedMemorySize, K2_LDS);
  fnet_k1<<<256, 512, K1_LDS, stream>>>(x, Wr, Wi, w1, b1, w2, BT, H, cnt, Cr, Ci);
  fnet_k2<<<256, 512, K2_LDS, stream>>>(Cr, Ci, H, BT, x, b2, out, slabs, cnt);
}
```
